```python
import torch

def _softmax(x, axis=-1):
    return torch.softmax(x, dim=axis)
def _gelu(x):
    return torch.nn.functional.gelu(x, approximate="tanh")
def _silu(x):
    return torch.nn.functional.silu(x)
def _sigmoid(x):
    return torch.sigmoid(x)
def _layernorm(x, axis=-1, eps=1e-5):
    m = x.mean(dim=axis, keepdim=True)
    v = x.var(dim=axis, keepdim=True, unbiased=False)
    return (x - m) / torch.sqrt(v + eps)
def _scatter_add(tgt, idx, src, axis=0):
    return tgt.clone().index_add_(axis, idx, src)
def _segment_sum(src, seg, n):
    out = torch.zeros((n,) + src.shape[1:], dtype=src.dtype)
    return out.index_add_(0, seg, src)
def _pad(x, pw):
    flat = tuple(v for pair in reversed(pw) for v in pair)
    return torch.nn.functional.pad(x, flat)

kernel_name = 'parallel_sum__spectral_gfnet__glu_gated_geglu__prompt'

def setup_inputs():

    _w = (lambda a: (lambda t: t.to(torch.bfloat16) if t.is_floating_point() else t)(torch.as_tensor(a)))
    def _A_setup():
        B, S, D = 1, 2048, 1024
        Df = D // 2 + 1
        return {
            "x": _w((lambda *s: torch.randn(*s, dtype=torch.bfloat16) * 0.05)(B, S, D)),
            "w_r": _w((lambda *s: torch.randn(*s, dtype=torch.bfloat16) * 0.8)(S, Df) * 0.02),
            "w_i": _w((lambda *s: torch.randn(*s, dtype=torch.bfloat16) * 0.8)(S, Df) * 0.02),
        }
    _A_in = _A_setup()

    def _B_setup():
        B, S, Dm, Df = 1, 2048, 1024, 3584
        return {
            "x": _w((lambda *s: torch.randn(*s, dtype=torch.bfloat16) * 0.05)(B, S, Dm)),
            "w_gate": _w((lambda *s: torch.randn(*s, dtype=torch.bfloat16) * 0.4)(Dm, Df) * (Dm ** -0.5)),
            "w_up": _w((lambda *s: torch.randn(*s, dtype=torch.bfloat16) * 0.4)(Dm, Df) * (Dm ** -0.5)),
            "w_down": _w((lambda *s: torch.randn(*s, dtype=torch.bfloat16) * 0.4)(Df, Dm) * (Df ** -0.5)),
        }
    _B_in = _B_setup()

    return {
        "A_x": _A_in["x"],
        "A_w_r": _A_in["w_r"],
        "A_w_i": _A_in["w_i"],
        "B_x": _B_in["x"],
        "B_w_gate": _B_in["w_gate"],
        "B_w_up": _B_in["w_up"],
        "B_w_down": _B_in["w_down"],
    }

def reference(A_x, A_w_r, A_w_i, B_x, B_w_gate, B_w_up, B_w_down):
    def _A_ref(x, w_r, w_i):
        B, S, D = x.shape
        X = (lambda x, axes=(-2,-1): torch.fft.rfft2(x.float(), dim=axes))(x, axes=(-2, -1))
        W = w_r + 1j * w_i
        X = X * W
        y = (lambda x, s, axes=(-2,-1): torch.fft.irfft2(x, s=s, dim=axes).to(torch.bfloat16))(X, (S, D), axes=(-2, -1))
        return y

    def _B_ref(x, w_gate, w_up, w_down):
        g = torch.einsum("bsd,df->bsf", x, w_gate)
        u = torch.einsum("bsd,df->bsf", x, w_up)
        h = _gelu(g) * u
        return torch.einsum("bsf,fd->bsd", h, w_down)

    x = A_x
    xn = _layernorm(x, axis=-1)
    return x + _A_ref(xn, A_w_r, A_w_i) + _B_ref(xn, B_w_gate, B_w_up, B_w_down)
```

```cpp
#include <hip/hip_runtime.h>
#include <hip/hip_bf16.h>
#include <stdint.h>
typedef unsigned short u16;
__device__ __forceinline__ float bf2f(u16 b){ return __uint_as_float(((unsigned)b)<<16); }
__device__ __forceinline__ u16 f2bf(float f){ unsigned u=__float_as_uint(f); u += 0x7fffu + ((u>>16)&1u); return (u16)(u>>16); }
__device__ __forceinline__ float rbf(float f){ return bf2f(f2bf(f)); }
constexpr int S=2048, D=1024, DF=513, F=3584;

__global__ void ln_k(const u16* x, u16* xn){
  int row=blockIdx.x, t=threadIdx.x; __shared__ float red[256];
  float v[4]; float s=0;
  for(int i=0;i<4;i++){ v[i]=bf2f(x[row*D+t+256*i]); s+=v[i]; }
  red[t]=s; __syncthreads();
  for(int o=128;o>0;o>>=1){ if(t<o) red[t]+=red[t+o]; __syncthreads(); }
  float mean=red[0]/D; __syncthreads();
  float q=0; for(int i=0;i<4;i++){ float d=v[i]-mean; q+=d*d; }
  red[t]=q; __syncthreads();
  for(int o=128;o>0;o>>=1){ if(t<o) red[t]+=red[t+o]; __syncthreads(); }
  float var=red[0]/D;
  float m=rbf(mean), vb=rbf(var); float sd=rbf(sqrtf(rbf(vb+1e-5f)));
  for(int i=0;i<4;i++) xn[row*D+t+256*i]=f2bf(rbf(v[i]-m)/sd);
}
__global__ void rowdft(const u16* xn, float2* X1){
  int s=blockIdx.x; __shared__ float xs[D]; __shared__ float2 tw[D];
  for(int i=threadIdx.x;i<D;i+=blockDim.x){ xs[i]=bf2f(xn[s*D+i]); float sn,cs; sincospif(-2.0f*i/D,&sn,&cs); tw[i]=make_float2(cs,sn);} __syncthreads();
  for(int k=threadIdx.x;k<DF;k+=blockDim.x){ float re=0,im=0; for(int n=0;n<D;n++){ float2 w=tw[(n*k)&(D-1)]; re+=xs[n]*w.x; im+=xs[n]*w.y;} X1[s*DF+k]=make_float2(re,im);} }
__global__ void colconv(const float2* X1, const u16* wr, const u16* wi, float2* Z){
  int c=blockIdx.x; __shared__ float2 a[S]; __shared__ float2 b[S]; __shared__ float2 tw[S];
  for(int i=threadIdx.x;i<S;i+=blockDim.x){ a[i]=X1[i*DF+c]; float sn,cs; sincospif(-2.0f*i/S,&sn,&cs); tw[i]=make_float2(cs,sn);} __syncthreads();
  for(int k=threadIdx.x;k<S;k+=blockDim.x){ float re=0,im=0; for(int n=0;n<S;n++){ float2 w=tw[(n*k)&(S-1)]; float2 v=a[n]; re+=v.x*w.x-v.y*w.y; im+=v.x*w.y+v.y*w.x;}
    float fr=bf2f(wr[k*DF+c]), fi=bf2f(wi[k*DF+c]); b[k]=make_float2(re*fr-im*fi, re*fi+im*fr);} __syncthreads();
  for(int n=threadIdx.x;n<S;n+=blockDim.x){ float re=0,im=0; for(int k=0;k<S;k++){ float2 w=tw[(n*k)&(S-1)]; float2 v=b[k]; re+=v.x*w.x+v.y*w.y; im+=-v.x*w.y+v.y*w.x;} Z[n*DF+c]=make_float2(re,im);} }
__global__ void rowidft(const float2* Z, const u16* x, u16* t){
  int s=blockIdx.x; __shared__ float2 zs[DF]; __shared__ float2 tw[D];
  for(int i=threadIdx.x;i<D;i+=blockDim.x){ float sn,cs; sincospif(2.0f*i/D,&sn,&cs); tw[i]=make_float2(cs,sn);}
  for(int i=threadIdx.x;i<DF;i+=blockDim.x) zs[i]=Z[s*DF+i]; __syncthreads();
  for(int n=threadIdx.x;n<D;n+=blockDim.x){ float acc=zs[0].x + ((n&1)?-zs[512].x:zs[512].x);
    for(int k=1;k<512;k++){ float2 w=tw[(n*k)&(D-1)]; acc+=2.f*(zs[k].x*w.x-zs[k].y*w.y);}
    float y=rbf(acc*(1.0f/(S*(float)D))); t[s*D+n]=f2bf(bf2f(x[s*D+n])+y);} }
template<int MODE> __global__ void gemm(const u16* A, const u16* B0, const u16* B1, const u16* T, u16* C, int M,int N,int K){
  __shared__ float As[16][65], Bs[16][65], Bs1[16][65];
  int tx=threadIdx.x&15, ty=threadIdx.x>>4; int m0=blockIdx.y*64, n0=blockIdx.x*64;
  float acc[4][4]={}, acc1[4][4]={};
  for(int k0=0;k0<K;k0+=16){
    for(int i=threadIdx.x;i<1024;i+=256){ int r=i>>4,c=i&15; As[c][r]=bf2f(A[(m0+r)*K+k0+c]); int kr=i>>6, nc=i&63; Bs[kr][nc]=bf2f(B0[(k0+kr)*N+n0+nc]); if(MODE==0) Bs1[kr][nc]=bf2f(B1[(k0+kr)*N+n0+nc]); }
    __syncthreads();
    for(int k=0;k<16;k++){ for(int i=0;i<4;i++) for(int j=0;j<4;j++){ acc[i][j]+=As[k][ty*4+i]*Bs[k][tx*4+j]; if(MODE==0) acc1[i][j]+=As[k][ty*4+i]*Bs1[k][tx*4+j]; } }
    __syncthreads(); }
  for(int i=0;i<4;i++) for(int j=0;j<4;j++){ int m=m0+ty*4+i,n=n0+tx*4+j;
    if(MODE==0){ float g=rbf(acc[i][j]), u=rbf(acc1[i][j]); float z=0.7978845608f*(g+0.044715f*g*g*g); float ge=rbf(0.5f*g*(1.f+tanhf(z))); C[m*N+n]=f2bf(ge*u);}
    else { C[m*N+n]=f2bf(bf2f(T[m*N+n])+rbf(acc[i][j])); } } }

extern "C" void kernel_launch(void* const* d_in, const int* in_sizes, int n_in, void* d_out, int out_size, void* d_ws, size_t ws_size, hipStream_t stream){
  const u16* x=(const u16*)d_in[0]; const u16* wr=(const u16*)d_in[1]; const u16* wi=(const u16*)d_in[2];
  const u16* wg=(const u16*)d_in[4]; const u16* wu=(const u16*)d_in[5]; const u16* wd=(const u16*)d_in[6];
  char* ws=(char*)d_ws; u16* xn=(u16*)ws; float2* X1=(float2*)(ws+(8<<20)); float2* Z=(float2*)(ws+(24<<20)); u16* t=(u16*)(ws+(40<<20)); u16* h=(u16*)(ws+(48<<20));
  ln_k<<<S,256,0,stream>>>(x,xn);
  rowdft<<<S,256,0,stream>>>(xn,X1);
  colconv<<<DF,1024,0,stream>>>(X1,wr,wi,Z);
  rowidft<<<S,256,0,stream>>>(Z,x,t);
  gemm<0><<<dim3(F/64,S/64),256,0,stream>>>(xn,wg,wu,nullptr,h,S,F,D);
  gemm<1><<<dim3(D/64,S/64),256,0,stream>>>(h,wd,nullptr,t,(u16*)d_out,S,D,F);
}
```

```cpp
#include <hip/hip_runtime.h>
#include <stdint.h>

#define LAS __attribute__((address_space(3)))
#define GAS __attribute__((address_space(1)))
typedef short bf16x8 __attribute__((ext_vector_type(8)));
typedef float f32x4 __attribute__((ext_vector_type(4)));
typedef float f32x16 __attribute__((ext_vector_type(16)));
typedef unsigned u32x4 __attribute__((ext_vector_type(4)));
typedef unsigned u32x2 __attribute__((ext_vector_type(2)));
typedef u32x2 u32x2u __attribute__((aligned(2)));
typedef __bf16 bf16x2v __attribute__((ext_vector_type(2)));
typedef _Float16 h16x2 __attribute__((ext_vector_type(2)));
typedef float f32x2 __attribute__((ext_vector_type(2)));

constexpr int S_ = 2048, D_ = 1024, F_ = 3584;
constexpr int OFF_E = 0, OFF_F1 = 1024, OFF_F2 = 2048, OFF_F3 = 2560, OFF_FH = 4096, OFF_FP = 8192, OFF_R = 32768, OFF_FF = 65536;
constexpr int SLAB = 135424;
constexpr int OFF_X1 = 1 << 20, OFF_WT = 6 << 20, OFF_Z = 11 << 20, OFF_T = 16 << 20, OFF_P = 20 << 20, OFF_H = 24 << 20, WS_USED = 40 << 20;
constexpr int L_EX = 131072, L_TW = L_EX, L_FR = L_EX + 12288, L_SC = L_EX + 20480, LDS_BYTES = 163840;
constexpr int NA = 32;
constexpr unsigned SPIN_CAP = 1u << 18;

__device__ __forceinline__ unsigned pk_bf16(float lo, float hi) { f32x2 v = {lo, hi}; return __builtin_bit_cast(unsigned, __builtin_convertvector(v, bf16x2v)); }
__device__ __forceinline__ unsigned pk_f16(float lo, float hi) { h16x2 v = {(_Float16)lo, (_Float16)hi}; return __builtin_bit_cast(unsigned, v); }
__device__ __forceinline__ float bf_lo(unsigned u) { return __uint_as_float(u << 16); }
__device__ __forceinline__ float bf_hi(unsigned u) { return __uint_as_float(u & 0xffff0000u); }
__device__ __forceinline__ float rbf(float f) { return bf_lo(pk_bf16(f, f)); }
__device__ __forceinline__ float cosr(float rev) { return __builtin_amdgcn_cosf(rev); }
__device__ __forceinline__ float dot2(unsigned a, unsigned b, float c) { return __builtin_amdgcn_fdot2_f32_bf16(__builtin_bit_cast(bf16x2v, a), __builtin_bit_cast(bf16x2v, b), c, false); }
__device__ __forceinline__ void cmul(float re, float im, unsigned t, float& r2, float& i2) {
  h16x2 tw = __builtin_bit_cast(h16x2, t);
  float a = __builtin_fmaf(im, (float)tw[1], 0.0f), b = __builtin_fmaf(im, (float)tw[0], 0.0f);
  r2 = __builtin_fmaf(re, (float)tw[0], -a); i2 = __builtin_fmaf(re, (float)tw[1], b);
  asm("" : "+v"(r2)); asm("" : "+v"(i2));
}
template <int CTRL> __device__ __forceinline__ float dppf(float v) { return __builtin_bit_cast(float, __builtin_amdgcn_update_dpp(0, __builtin_bit_cast(int, v), CTRL, 0xf, 0xf, true)); }
__device__ __forceinline__ float par_sum(float v) {
  v += dppf<0x4E>(v); v += dppf<0x124>(v); v += dppf<0x128>(v);
  auto a = __builtin_amdgcn_permlane16_swap(__float_as_uint(v), __float_as_uint(v), false, false); v = __uint_as_float(a[0]) + __uint_as_float(a[1]);
  auto b = __builtin_amdgcn_permlane32_swap(__float_as_uint(v), __float_as_uint(v), false, false); return __uint_as_float(b[0]) + __uint_as_float(b[1]);
}
__device__ __forceinline__ float rdl(float v, int l) { return __builtin_bit_cast(float, __builtin_amdgcn_readlane(__builtin_bit_cast(int, v), l)); }

__device__ __forceinline__ int opq(int v) { asm volatile("" : "+v"(v)); return v; }
__device__ __forceinline__ int lane_id() { int v; asm volatile("v_mbcnt_lo_u32_b32 %0, -1, 0\n\tv_mbcnt_hi_u32_b32 %0, -1, %0" : "=v"(v)); __builtin_assume(v >= 0 && v < 64); return v; }
#define LANES const int lane = lane_id(), tid = wid * 64 + lane, l31 = lane & 31, hh = lane >> 5, fr = lane & 15, fq = lane >> 4; (void)tid; (void)l31; (void)hh; (void)fr; (void)fq
#define BC8(x) __builtin_bit_cast(bf16x8, x)
#define MF32(a, b, c) __builtin_amdgcn_mfma_f32_32x32x16_bf16(BC8(a), BC8(b), c, 0, 0, 0)
#define MF16(a, b, c) __builtin_amdgcn_mfma_f32_16x16x32_bf16(BC8(a), BC8(b), c, 0, 0, 0)
#define WAIT_V(n) asm volatile("s_waitcnt vmcnt(" #n ")" ::: "memory")
#define WAIT_L(n) asm volatile("s_waitcnt lgkmcnt(" #n ")" ::: "memory")
#define WAIT_ALL do { __builtin_amdgcn_s_waitcnt(0x0070); asm volatile("" ::: "memory"); } while (0)
#define BAR __builtin_amdgcn_s_barrier()
#define SCHED __builtin_amdgcn_sched_barrier(0)
#define UNR _Pragma("unroll")
#define LDS4(off) (*(LAS u32x4*)(lds + (off)))
#define LDS1(off) (*(LAS unsigned*)(lds + (off)))
#define ST_SC1(v, off) __builtin_amdgcn_raw_buffer_store_b128(v, rsW, off, 0, 16)
#define LD_SC1(off) __builtin_amdgcn_raw_buffer_load_b128(rsW, off, 0, 16)
#define FLAG_ST(off) __hip_atomic_store((GAS unsigned*)(ws + (off)), epoch, __ATOMIC_RELAXED, __HIP_MEMORY_SCOPE_AGENT)
#define FLAG_LD(off) __hip_atomic_load((GAS unsigned*)(ws + (off)), __ATOMIC_RELAXED, __HIP_MEMORY_SCOPE_AGENT)
#define POLL1(off) do { for (unsigned sp_ = 0; sp_ < SPIN_CAP; ++sp_) { if (__all(FLAG_LD(off) == epoch)) break; __builtin_amdgcn_s_sleep(1); } \
    __builtin_amdgcn_fence(__ATOMIC_ACQUIRE, "wavefront"); } while (0)
#define TRR(dst, base, off) asm volatile("ds_read_b64_tr_b16 %0, %1 offset:%2" : "=v"(dst) : "v"(base), "i"(off) : "memory")
#define CAT(x) __builtin_shufflevector(x[0], x[1], 0, 1, 2, 3)
#define STAMP(i)
static const f32x16 ZERO16 = {0, 0, 0, 0, 0, 0, 0, 0, 0, 0, 0, 0, 0, 0, 0, 0};

__global__ __launch_bounds__(512, 2) void fused_k(const char* __restrict__ X, const char* __restrict__ Wr, const char* __restrict__ Wi,
                                                  const char* __restrict__ Wg, const char* __restrict__ Wu, const char* __restrict__ Wd,
                                                  char* __restrict__ O, char* ws) {
  extern __shared__ __attribute__((aligned(16))) char smem_[];
  LAS char* lds = (LAS char*)smem_;
  const int b = blockIdx.x, xg = b & 7, rr = b >> 3;
  const bool isG = rr < 28;
  const int g = xg * 4 + (rr & 3);
  const int wid = __builtin_amdgcn_readfirstlane(threadIdx.x >> 6);
  const auto rsW = __builtin_amdgcn_make_buffer_rsrc(ws, 0, WS_USED, 0x00020000);
  STAMP(0);

  const int pm = (xg & 1) * 4 + (rr & 3), pn = (xg >> 1) * 7 + (rr >> 2);
  const int wr = wid >> 2, wc = wid & 3;
  const int g28 = g < 28 ? g : g - 28;
  const char* cA = isG ? X + pm * 256 * (D_ * 2) : Wd + (g >> 3) * 256 * (D_ * 2) + (g & 7) * 256;
  const char* cG = isG ? Wg + pn * 256 : Wd + 384 * (F_ * 2) + g28 * 256;
  const char* cU = isG ? Wu + pn * 256 : Wd + 512 * (F_ * 2) + g28 * 256;
  const unsigned ldsw = wid * 1024;
#define G1_VOFF unsigned voffA, voffB; { int row = wid * 8 + (lane >> 3); voffA = row * (D_ * 2) + (((lane & 7) ^ (lane >> 3)) * 16); } \
  { int row = wid * 4 + fq; int swz = ((row & 3) | ((row >> 1) & 4)) << 1; voffB = row * (F_ * 2) + ((fr ^ swz) * 16); }
#define SA(bf, h) (((bf) * 2 + (h)) * 16384)
#define SB(bf, h) ((4 + (bf) * 2 + (h)) * 16384)
#define STA(bufoff, h, t) do { const char* g_ = cA + (h) * 128 * (D_ * 2) + (t) * 128; UNR for (int i_ = 0; i_ < 2; ++i_) \
    __builtin_amdgcn_global_load_lds((const unsigned*)(g_ + i_ * 64 * (D_ * 2) + voffA), (LAS unsigned*)(lds + (bufoff) + ldsw + i_ * 8192), 16, 0, 0); } while (0)
#define STB(bufoff, base, t) do { const char* g_ = (base) + (size_t)(t) * 64 * (F_ * 2); UNR for (int i_ = 0; i_ < 2; ++i_) \
    __builtin_amdgcn_global_load_lds((const unsigned*)(g_ + i_ * 32 * (F_ * 2) + voffB), (LAS unsigned*)(lds + (bufoff) + ldsw + i_ * 8192), 16, 0, 0); } while (0)
  unsigned epoch;
  {
  LANES;
  const int s = b * 8 + wid, sq = (wid >> 1) & 3, se = b & 3;
  unsigned e0, w5r, w5i; u32x4 xa[2]; u32x2 wrv[2], wiv[2];
#define GLD(dst, ins, voff, sbase, mod) asm volatile(ins " %0, %1, %2 " mod : "=v"(dst) : "v"(voff), "s"(sbase) : "memory")
  { const int xo_ = l31 * 64 + hh * 16, wo_ = ((lane >> 2) & 7) * 8 + (lane & 3) * 64 + hh * 256;
    GLD(xa[0], "global_load_dwordx4", xo_, X + s * 2048, ""); GLD(xa[1], "global_load_dwordx4", xo_, X + s * 2048, "offset:32");
    GLD(e0, "global_load_dword", 0, ws + OFF_E, "sc1");
    GLD(wrv[0], "global_load_dwordx2", wo_, Wr + s * 1026, ""); GLD(wiv[0], "global_load_dwordx2", wo_, Wi + s * 1026, "");
    GLD(wrv[1], "global_load_dwordx2", wo_, Wr + s * 1026, "offset:512"); GLD(wiv[1], "global_load_dwordx2", wo_, Wi + s * 1026, "offset:512");
    GLD(w5r, "global_load_ushort", 0, Wr + s * 1026, "offset:1024"); GLD(w5i, "global_load_ushort", 0, Wi + s * 1026, "offset:1024"); }
  G1_VOFF;
  SCHED;
  STB(SB(0, 0), cG, 0); STA(SA(0, 0), 0, 0); STB(SB(0, 1), cU, 0); STA(SA(0, 1), 1, 0);
  STB(SB(1, 0), cG, 1); STA(SA(1, 0), 0, 1); STB(SB(1, 1), cU, 1); STA(SA(1, 1), 1, 1);
  SCHED;

  {
    const int e2 = ((l31 >> 1) & 1) + 2 * ((l31 >> 3) & 1), k2 = 4 * (2 * (l31 >> 4) + ((l31 >> 2) & 1)) + (e2 ^ se);
    const float mult = (float)(wid < 4 ? l31 : k2) * (1.0f / 32);
    const float ph = 0.25f * (float)(wid < 4 ? (wid >> 1) : (l31 & 1) - ((wid >> 1) & 1));
    const float b0 = (float)(16 * (wid & 1) + 4 * hh);
    float v[8];
    UNR for (int e = 0; e < 8; e++) v[e] = cosr(__builtin_fmaf(b0 + (float)((e & 3) + 8 * (e >> 2)), mult, ph));
    LDS4(L_SC + wid * 1024 + lane * 16) = (u32x4){pk_bf16(v[0], v[1]), pk_bf16(v[2], v[3]), pk_bf16(v[4], v[5]), pk_bf16(v[6], v[7])};
    const float a0 = (float)(2 * (wid & 1) + 8 * (wid >> 1) + 4 * hh), m2 = (float)l31 * (1.0f / 1024);
    const float t0 = a0 * m2, t1 = t0 + m2;
    *(LAS u32x2*)(lds + L_SC + 8192 + (wid >> 1) * 1024 + lane * 16 + (wid & 1) * 8) = (u32x2){pk_f16(cosr(t0), cosr(t0 + 0.25f)), pk_f16(cosr(t1), cosr(t1 + 0.25f))};
  }
  WAIT_L(0); BAR;
  {
    u32x4 tb[12];
    UNR for (int i = 0; i < 12; i++) tb[i] = LDS4(L_SC + i * 1024 + lane * 16);
    u32x4 idm[2];
    UNR for (int j = 0; j < 2; j++) { const int d = l31 - 16 * j - 8 * hh;
      UNR for (int p = 0; p < 4; p++) idm[j][p] = (d >> 1) == p ? (0x3f80u << (16 * (d & 1))) : 0u; }
    asm volatile("s_waitcnt vmcnt(23)" : "+v"(xa[0]), "+v"(xa[1])::"memory");
    f32x16 xt = MF32(xa[0], idm[0], ZERO16); xt = MF32(xa[1], idm[1], xt);
    float s0 = 0.f, s1 = 0.f;
    UNR for (int j = 0; j < 2; j++) UNR for (int p = 0; p < 4; p++) { s0 = dot2(xa[j][p], 0x3f803f80u, s0); s1 = dot2(xa[j][p], 0xbf803f80u, s1); }
    const float sv = par_sum(((lane & 1) ? s1 : s0) + dppf<0xB1>((lane & 1) ? s0 : s1));
    const float mu = rdl(sv, 0) * (1.0f / D_), alt = rdl(sv, 1), m = rbf(mu);
    float q = 0.f;
    UNR for (int i = 0; i < 16; i++) { xt[i] -= m; q = __builtin_fmaf(xt[i], xt[i], q); }
    u32x4 df[2];
    UNR for (int i = 0; i < 8; i++) df[i >> 2][i & 3] = pk_bf16(xt[2 * i], xt[2 * i + 1]);
    f32x16 tr = MF32(df[0], tb[0], ZERO16), ti = MF32(df[0], tb[2], ZERO16);
    tr = MF32(df[1], tb[1], tr); ti = MF32(df[1], tb[3], ti);
    q = par_sum(q + dppf<0xB1>(q));
    const float var = fmaxf(q * (1.0f / D_) - (mu - m) * (mu - m), 0.f);
    const float sd = rbf(__builtin_amdgcn_sqrtf(rbf(rbf(var) + 1e-5f))), rs = __builtin_amdgcn_rcpf(sd);
    u32x4 br[2], bi[2];
    UNR for (int i = 0; i < 8; i++) { float r0, i0, r1, i1;
      cmul(tr[2 * i], ti[2 * i], tb[8 + (i >> 1)][(2 * i) & 3], r0, i0); cmul(tr[2 * i + 1], ti[2 * i + 1], tb[8 + (i >> 1)][(2 * i + 1) & 3], r1, i1);
      br[i >> 2][i & 3] = pk_bf16(r0, r1); bi[i >> 2][i & 3] = pk_bf16(i0, i1); }
    f32x16 xo = MF32(tb[4], br[0], ZERO16); xo = MF32(tb[6], bi[0], xo); xo = MF32(tb[5], br[1], xo); xo = MF32(tb[7], bi[1], xo);
    UNR for (int e = 0; e < 4; e++) xo[2 * e + 1] = (lane == 0 && se == e) ? alt : xo[2 * e + 1];
    asm volatile("s_waitcnt vmcnt(16)" : "+v"(e0), "+v"(wrv[0]), "+v"(wrv[1]), "+v"(wiv[0]), "+v"(wiv[1]), "+v"(w5r), "+v"(w5i)::"memory");
    epoch = __builtin_amdgcn_readfirstlane(e0) * 0x9E3779B1u + 0x7F4A7C15u;
    const int xoff = OFF_X1 + ((l31 >> 2) * 4 + hh) * SLAB + s * 64 + (((lane & 3) ^ sq) * 16);
    UNR for (int u = 0; u < 2; u++) { u32x4 w;
      UNR for (int e = 0; e < 4; e++) w[e] = pk_bf16(xo[8 * u + 2 * e] * rs, xo[8 * u + 2 * e + 1] * rs);
      ST_SC1(w, xoff + 2 * u * SLAB); }
    const unsigned sel0 = (se & 1) ? 0x07060302u : 0x05040100u, sel1 = sel0 ^ 0x02020202u;
    UNR for (int u = 0; u < 2; u++) { const unsigned ra = (se & 2) ? wrv[u][1] : wrv[u][0], rb = (se & 2) ? wrv[u][0] : wrv[u][1];
      const unsigned ia = (se & 2) ? wiv[u][1] : wiv[u][0], ib = (se & 2) ? wiv[u][0] : wiv[u][1];
      u32x4 w = {__builtin_amdgcn_perm(ia, ra, sel0), __builtin_amdgcn_perm(ia, ra, sel1), __builtin_amdgcn_perm(ib, rb, sel0), __builtin_amdgcn_perm(ib, rb, sel1)};
      ST_SC1(w, xoff + (OFF_WT - OFF_X1) + 2 * u * SLAB); }
    u32x4 rec = {pk_bf16(m, sd), epoch, w5r | (w5i << 16), __builtin_amdgcn_perm(wiv[0][0], wrv[0][0], 0x05040100u)};
    ST_SC1(rec, lane == 0 ? OFF_R + s * 16 : WS_USED);
  }
  }
  SCHED;
  STAMP(1);

  const int half = xg & 1, tm = (xg >> 1) * 4 + (rr & 3), tn = rr >> 2, tile = tm * 8 + tn;
  const int kt0 = half ? NA : 0, nkt = half ? 56 - NA : NA;
  const char* cB = Wd + (size_t)kt0 * 64 * (D_ * 2) + tn * 256;
#define G2_VOFF unsigned voffD; { int row = wid * 4 + fq; voffD = row * (D_ * 2) + ((fr ^ ((row & 7) << 1)) * 16); }
#define STD1(t, i) __builtin_amdgcn_global_load_lds((const unsigned*)(cB + (size_t)(t) * 64 * (D_ * 2) + (i) * 32 * (D_ * 2) + voffD), (LAS unsigned*)(lds + (((t) & 7) * 16384) + ldsw + (i) * 8192), 16, 0, 0)
#define G2_PRELOAD do { G2_VOFF; UNR for (int t_ = 0; t_ < 8; t_++) { STD1(t_, 0); STD1(t_, 1); } } while (0)

  if (isG) {
    LANES; G1_VOFF;
    const int aoffx = (wr * 64 + fr) * 128 + ((fq ^ (fr & 7)) * 16);
    const int aoff0 = aoffx ^ (wr * 64), aoff1 = aoff0 ^ 64;
    int bo00, bo01, bo10, bo11;
    { int q = fr >> 2, p = lane & 3; int row = 8 * fq + q; int swz = ((row & 3) | ((row >> 1) & 4)) << 1;
      int bb = row * 256 + (((4 * wc + (p >> 1)) ^ swz) * 16) + 8 * (p & 1) + 65536;
      bo00 = bb + wr * 8192; bo01 = bo00 ^ 8192; bo10 = bo00 ^ 32; bo11 = bo01 ^ 32; }
#define LDA(dst, bf, h) do { UNR for (int m = 0; m < 4; ++m) { dst[m][0] = LDS4(SA(bf, h) + aoff0 + m * 2048); dst[m][1] = LDS4(SA(bf, h) + aoff1 + m * 2048); } } while (0)
#define LDB(dst, bf, h) do { TRR(dst[0][0][0], bo00, SB(bf, h) - 65536); TRR(dst[0][0][1], bo00, SB(bf, h) - 65536 + 1024); \
    TRR(dst[1][0][0], bo10, SB(bf, h) - 65536); TRR(dst[1][0][1], bo10, SB(bf, h) - 65536 + 1024); \
    TRR(dst[0][1][0], bo01, SB(bf, h) - 65536); TRR(dst[0][1][1], bo01, SB(bf, h) - 65536 + 1024); \
    TRR(dst[1][1][0], bo11, SB(bf, h) - 65536); TRR(dst[1][1][1], bo11, SB(bf, h) - 65536 + 1024); } while (0)
#define WAIT_LB(B) asm volatile("s_waitcnt lgkmcnt(0)" : "+v"(B[0][0][0]), "+v"(B[0][0][1]), "+v"(B[0][1][0]), "+v"(B[0][1][1]), "+v"(B[1][0][0]), "+v"(B[1][0][1]), "+v"(B[1][1][0]), "+v"(B[1][1][1])::"memory")
#define MMA(ai, bj, Bt) do { __builtin_amdgcn_s_setprio(1); UNR for (int i = 0; i < 16; ++i) { const int m = i >> 2, n = ((i >> 1) ^ (i >> 2)) & 1, k = (i ^ (i >> 1)) & 1; \
    acc[ai][bj][m][n] = MF16(CAT(Bt[n][k]), At[m][k], acc[ai][bj][m][n]); SCHED; } __builtin_amdgcn_s_setprio(0); } while (0)
#define CSUM(bj, Bt) do { UNR for (int n = 0; n < 2; n++) UNR for (int i = 0; i < 4; i++) cs[bj][n] = dot2(Bt[n][0][i >> 1][i & 1], 0x3f803f80u, cs[bj][n]); SCHED; } while (0)
    f32x4 acc[2][2][4][2];
    UNR for (int a = 0; a < 2; a++) UNR for (int c = 0; c < 2; c++) UNR for (int m = 0; m < 4; m++) UNR for (int n = 0; n < 2; n++) acc[a][c][m][n] = (f32x4){0, 0, 0, 0};
    float cs[2][2] = {{0.f, 0.f}, {0.f, 0.f}};
    u32x4 At[4][2]; u32x2 B0[2][2][2], B1[2][2][2];
    WAIT_V(13); BAR;
    if (wr == 1) BAR;
    LDB(B0, 0, 0); SCHED; LDA(At, 0, 0);
    WAIT_L(8); BAR; WAIT_LB(B0); MMA(0, 0, B0); BAR; SCHED;
    LDB(B1, 0, 1); CSUM(0, B0);
    BAR; WAIT_LB(B1); MMA(0, 1, B1); BAR;
    LDA(At, 0, 1);
    BAR; WAIT_L(0); MMA(1, 0, B0); BAR; SCHED;
    CSUM(1, B1); WAIT_V(0); STB(SB(0, 0), cG, 2); STA(SA(0, 0), 0, 2); STB(SB(0, 1), cU, 2);
    BAR; MMA(1, 1, B1); BAR;
    if (tid == 0) FLAG_ST(OFF_F1 + b * 4);
#define KTILE(bf, t) do { \
    LDB(B0, bf, 0); SCHED; LDA(At, bf, 0); STA(SA(1 - bf, 1), 1, (t) + 1); \
    WAIT_L(8); BAR; WAIT_LB(B0); MMA(0, 0, B0); BAR; SCHED; \
    LDB(B1, bf, 1); STB(SB(bf, 0), cG, (t) + 2); CSUM(0, B0); \
    BAR; WAIT_LB(B1); MMA(0, 1, B1); BAR; \
    LDA(At, bf, 1); STA(SA(bf, 0), 0, (t) + 2); \
    BAR; WAIT_L(0); MMA(1, 0, B0); BAR; SCHED; \
    STB(SB(bf, 1), cU, (t) + 2); CSUM(1, B1); \
    WAIT_V(6); BAR; MMA(1, 1, B1); BAR; } while (0)
    KTILE(1, 1);
    for (int t = 2; t < 14; t += 2) { KTILE(0, t); KTILE(1, t + 1); }
    LDB(B0, 0, 0); LDA(At, 0, 0); STA(SA(1, 1), 1, 15);
    BAR; WAIT_LB(B0); MMA(0, 0, B0); BAR;
    LDB(B1, 0, 1); CSUM(0, B0); BAR; WAIT_LB(B1); MMA(0, 1, B1); BAR;
    LDA(At, 0, 1); CSUM(1, B1); WAIT_V(4); BAR; WAIT_L(0); MMA(1, 0, B0); MMA(1, 1, B1); BAR;
    LDB(B0, 1, 0); LDA(At, 1, 0); WAIT_V(2); BAR; WAIT_LB(B0); MMA(0, 0, B0); BAR;
    LDB(B1, 1, 1); CSUM(0, B0); WAIT_V(0);
    GAS unsigned long long* recp = (GAS unsigned long long*)(ws + OFF_R + (pm * 256 + (tid & 255)) * 16);
    unsigned long long rec = __hip_atomic_load(recp, __ATOMIC_RELAXED, __HIP_MEMORY_SCOPE_AGENT);
    BAR; WAIT_LB(B1); MMA(0, 1, B1); BAR;
    LDA(At, 1, 1); CSUM(1, B1);
    UNR for (int c = 0; c < 2; c++) UNR for (int n = 0; n < 2; n++) { float v = cs[c][n];
      auto a = __builtin_amdgcn_permlane16_swap(__float_as_uint(v), __float_as_uint(v), false, false); v = __uint_as_float(a[0]) + __uint_as_float(a[1]);
      auto d = __builtin_amdgcn_permlane32_swap(__float_as_uint(v), __float_as_uint(v), false, false); cs[c][n] = __uint_as_float(d[0]) + __uint_as_float(d[1]); }
    LDS1(L_EX + 4096 + wr * 1024 + (fq >> 1) * 512 + (wc * 32 + (fq & 1) * 16 + fr) * 4) =
        __float_as_uint((fq & 2) ? ((fq & 1) ? cs[1][1] : cs[1][0]) : ((fq & 1) ? cs[0][1] : cs[0][0]));
    for (unsigned sp = 0; sp < SPIN_CAP && !__all((unsigned)(rec >> 32) == epoch); ++sp) {
      __builtin_amdgcn_s_sleep(1); rec = __hip_atomic_load(recp, __ATOMIC_RELAXED, __HIP_MEMORY_SCOPE_AGENT); }
    { const float m = bf_lo((unsigned)rec), sd = bf_hi((unsigned)rec), r = __builtin_amdgcn_rcpf(sd);
      const float c0 = -2.302208198f;
      *(LAS f32x4*)(lds + L_EX + (tid & 255) * 16) = (f32x4){-m, c0 * r, c0 * 0.044715f * r * r * r, sd * sd}; }
    BAR; WAIT_L(0); MMA(1, 0, B0); MMA(1, 1, B1); BAR;
    if (wr == 0) BAR;
    STAMP(2);
    f32x4 rc[8], cg[2], cu[2];
    UNR for (int i = 0; i < 8; i++) rc[i] = *(LAS f32x4*)(lds + L_EX + ((i >> 2) * 128 + wr * 64 + (i & 3) * 16 + fr) * 16);
    UNR for (int n = 0; n < 2; n++) { const int o = L_EX + 4096 + (wc * 32 + n * 16 + fq * 4) * 4;
      cg[n] = *(LAS f32x4*)(lds + o) + *(LAS f32x4*)(lds + o + 1024); cu[n] = *(LAS f32x4*)(lds + o + 512) + *(LAS f32x4*)(lds + o + 1536); }
    SCHED; WAIT_L(0);
    G2_PRELOAD;
    SCHED;
    const int hoff = OFF_H + ((pm * 16 + wr * 4) * 112 + pn * 4 + wc) * 1024 + lane * 16;
    UNR for (int i = 0; i < 8; i++) { float hv[8];
      UNR for (int j = 0; j < 8; j++) { const float gg = __builtin_fmaf(rc[i][0], cg[j >> 2][j & 3], acc[i >> 2][0][i & 3][j >> 2][j & 3]);
        const float uu = __builtin_fmaf(rc[i][0], cu[j >> 2][j & 3], acc[i >> 2][1][i & 3][j >> 2][j & 3]);
        const float ex = __builtin_amdgcn_exp2f(gg * __builtin_fmaf(gg * gg, rc[i][2], rc[i][1]));
        hv[j] = gg * uu * __builtin_amdgcn_rcpf(__builtin_fmaf(ex, rc[i][3], rc[i][3])); }
      ST_SC1(((u32x4){pk_bf16(hv[0], hv[1]), pk_bf16(hv[2], hv[3]), pk_bf16(hv[4], hv[5]), pk_bf16(hv[6], hv[7])}), hoff + ((i >> 2) * 8 + (i & 3)) * 112 * 1024); }
    STAMP(3);
  } else {
    LANES;
    WAIT_ALL; BAR;
    if (tid == 0) FLAG_ST(OFF_F1 + b * 4);
    UNR for (int z = 0; z < 2; z++) {
      const float mult = (float)(32 * (wid >> 2) + l31) * (1.0f / 64), b0 = (float)(16 * (wid & 3) + 4 * hh);
      float v[8];
      UNR for (int e = 0; e < 8; e++) v[e] = cosr(__builtin_fmaf(b0 + (float)((e & 3) + 8 * (e >> 2)), mult, -0.25f * z));
      LDS4((z * 8 + wid) * 1024 + lane * 16) = (u32x4){pk_bf16(v[0], v[1]), pk_bf16(v[2], v[3]), pk_bf16(v[4], v[5]), pk_bf16(v[6], v[7])}; }
    { const float mult = (float)l31 * (1.0f / 32), b0 = (float)(8 * (wid & 3) + 4 * hh), ph = 0.25f * (float)(wid >> 2);
      u32x4 w;
      UNR for (int e = 0; e < 4; e++) { const float a = __builtin_fmaf(b0 + (float)e, mult, ph); w[e] = pk_bf16(cosr(a), cosr(a - 0.25f)); }
      LDS4(L_FR + wid * 1024 + lane * 16) = w; }
    UNR for (int z = 0; z < 3; z++) {
      const float a0 = (float)(2 * (wid & 1) + 8 * (wid >> 1) + 4 * hh + (z == 1 ? 32 : 0)), m2 = (float)(l31 + (z == 2 ? 32 : 0)) * (1.0f / 2048);
      const float t0 = a0 * m2, t1 = t0 + m2;
      *(LAS u32x2*)(lds + L_TW + z * 4096 + (wid >> 1) * 1024 + lane * 16 + (wid & 1) * 8) = (u32x2){pk_f16(cosr(t0), cosr(t0 + 0.25f)), pk_f16(cosr(t1), cosr(t1 + 0.25f))}; }
    WAIT_L(0); BAR;
    u32x4 CC[8], SS[8];
    UNR for (int i = 0; i < 8; i++) { CC[i] = LDS4(i * 1024 + lane * 16); SS[i] = LDS4(8192 + i * 1024 + lane * 16); }
    if (wid == 0) {
      for (unsigned sp = 0; sp < SPIN_CAP; ++sp) { const u32x4 f = LD_SC1(OFF_F1 + lane * 16); asm volatile("" ::: "memory");
        if (__all(f[0] == epoch && f[1] == epoch && f[2] == epoch && f[3] == epoch)) break; __builtin_amdgcn_s_sleep(1); }
      if (g == 0 && lane == 0) FLAG_ST(OFF_E);
    }
    WAIT_L(0); BAR;
    STAMP(2);
    u32x4 xc[8], xo[8];
    {
      u32x4 sx[16], sw[16];
      const int so = g * SLAB + wid * 16384 + lane * 16;
      UNR for (int i = 0; i < 16; i++) sx[i] = LD_SC1(OFF_X1 + so + i * 1024);
      UNR for (int i = 0; i < 16; i++) sw[i] = LD_SC1(OFF_WT + so + i * 1024);
      UNR for (int i = 0; i < 16; i++) LDS4(wid * 16384 + i * 1024 + lane * 16) = sx[i];
      if (g == 0) {
        u32x4 f1, f2;
        UNR for (int e = 0; e < 4; e++) { const int j = l31 + 32 * (8 * wid + 4 * hh + e);
          const unsigned long long p = __hip_atomic_load((GAS unsigned long long*)(ws + OFF_R + j * 16 + 8), __ATOMIC_RELAXED, __HIP_MEMORY_SCOPE_AGENT);
          const unsigned long long n = __hip_atomic_load((GAS unsigned long long*)(ws + OFF_R + ((2048 - j) & 2047) * 16 + 8), __ATOMIC_RELAXED, __HIP_MEMORY_SCOPE_AGENT);
          const float ar = bf_lo(p >> 32) + bf_lo(n >> 32), ai = bf_hi(p >> 32) - bf_hi(n >> 32), br = bf_lo(p) + bf_lo(n), bi = bf_hi(p) - bf_hi(n);
          f1[e] = pk_bf16(0.125f * (ar + br), 0.125f * (ai + bi)); f2[e] = pk_bf16(0.125f * (ar - br), 0.125f * (ai - bi)); }
        *(u32x4*)(ws + OFF_FF + tid * 16) = f1; *(u32x4*)(ws + OFF_FF + 8192 + tid * 16) = f2;
      }
      WAIT_L(0); BAR;
#define XBASES const int xb0_ = l31 * 64 + hh * 16384 + ((((wid >> 1) ^ ((l31 >> 1) & 3)) * 4 + ((2 * (wid & 1)) ^ (l31 >> 3))) * 4); \
      const int xb[4] = {opq(xb0_), opq(xb0_ + 65536), opq(xb0_ ^ 4), opq((xb0_ ^ 4) + 65536)}
      XBASES;
      UNR for (int i = 0; i < 32; i++) { const int o = (i >> 4) * 2048 + ((i >> 2) & 1) * 32768 + (i & 3) * 4096;
        xc[i >> 2][i & 3] = LDS1(xb[(i >> 3) & 1] + o); xo[i >> 2][i & 3] = LDS1(xb[2 + ((i >> 3) & 1)] + o); }
      WAIT_L(0); BAR;
      UNR for (int i = 0; i < 16; i++) LDS4(wid * 16384 + i * 1024 + lane * 16) = sw[i];
      WAIT_V(0); WAIT_L(0); BAR;
    }
    STAMP(3);
#pragma unroll 1
    for (int c = 0; c < 2; c++) {
      LANES;
      const int ex = opq(L_EX + lane * 16) - L_EX;
      u32x4 pr[4], pi[4], pn_[4];
      { u32x4 fri[8];
        UNR for (int i = 0; i < 8; i++) fri[i] = LDS4(ex + (L_FR + i * 1024));
        UNR for (int mu = 0; mu < 2; mu++) { f32x16 re = ZERO16, im = ZERO16;
          UNR for (int k = 0; k < 4; k++) { re = MF32(xc[mu * 4 + k], fri[k], re); im = MF32(xc[mu * 4 + k], fri[4 + k], im); }
          UNR for (int q = 0; q < 4; q++) { const u32x4 tw = LDS4(ex + (L_TW + mu * 4096 + q * 1024)); float r[4], i[4];
            UNR for (int e = 0; e < 4; e++) cmul(re[4 * q + e], im[4 * q + e], tw[e], r[e], i[e]);
            UNR for (int e = 0; e < 2; e++) { pr[mu * 2 + (q >> 1)][(q & 1) * 2 + e] = pk_bf16(r[2 * e], r[2 * e + 1]); pi[mu * 2 + (q >> 1)][(q & 1) * 2 + e] = pk_bf16(i[2 * e], i[2 * e + 1]); } } } }
      SCHED;
      UNR for (int k = 0; k < 4; k++) pn_[k] = pr[k] ^ 0x80008000u;
      u32x4 yr[4], yi[4], yn[4];
      if (g == 0 && wid == 0 && c == 0) {
        f32x16 re[2], im[2];
        UNR for (int nu = 0; nu < 2; nu++) { re[nu] = ZERO16; im[nu] = ZERO16;
          UNR for (int k = 0; k < 4; k++) { re[nu] = MF32(CC[nu * 4 + k], pr[k], re[nu]); im[nu] = MF32(CC[nu * 4 + k], pi[k], im[nu]);
            re[nu] = MF32(SS[nu * 4 + k], pi[k], re[nu]); im[nu] = MF32(SS[nu * 4 + k], pn_[k], im[nu]); } }
        const int sb = opq(L_SC + l31 * 4 + hh * 512), mb = opq(L_SC + 8192 - 59 * 128 - l31 * 4 - hh * 512);
        UNR for (int i = 0; i < 32; i++) LDS1(sb + ((i & 3) + 8 * (i >> 2)) * 128) = pk_bf16(re[i >> 4][i & 15], im[i >> 4][i & 15]);
        if (lane == 0) LDS1(L_SC + 8192) = pk_bf16(re[0][0], im[0][0]);
        UNR for (int q = 0; q < 8; q++) { const u32x4 f1 = *(const u32x4*)(ws + OFF_FF + q * 1024 + lane * 16), f2 = *(const u32x4*)(ws + OFF_FF + 8192 + q * 1024 + lane * 16);
          float r[4], i[4];
          UNR for (int e = 0; e < 4; e++) { const unsigned mm = LDS1(mb + (59 - e - 8 * q) * 128);
            const float xr = re[q >> 2][(q & 3) * 4 + e], xi = im[q >> 2][(q & 3) * 4 + e], mr = bf_lo(mm), mi = bf_hi(mm);
            r[e] = xr * bf_lo(f1[e]) - xi * bf_hi(f1[e]) + mr * bf_lo(f2[e]) + mi * bf_hi(f2[e]);
            i[e] = -xr * bf_hi(f1[e]) - xi * bf_lo(f1[e]) - mr * bf_hi(f2[e]) + mi * bf_lo(f2[e]); }
          UNR for (int e = 0; e < 2; e++) { yr[q >> 1][(q & 1) * 2 + e] = pk_bf16(r[2 * e], r[2 * e + 1]); yi[q >> 1][(q & 1) * 2 + e] = pk_bf16(i[2 * e], i[2 * e + 1]); } SCHED; }
      } else {
        const int wb0 = l31 * 64 + hh * 8192 + ((((2 * (wid & 1) + c) ^ ((l31 >> 1) & 3)) * 4 + ((wid >> 1) ^ (l31 >> 3))) * 4);
        const int wb[2] = {opq(wb0), opq(wb0 + 65536)};
        UNR for (int nu = 0; nu < 2; nu++) { f32x16 re = ZERO16, im = ZERO16;
          UNR for (int k = 0; k < 4; k++) { re = MF32(CC[nu * 4 + k], pr[k], re); im = MF32(CC[nu * 4 + k], pi[k], im);
            re = MF32(SS[nu * 4 + k], pi[k], re); im = MF32(SS[nu * 4 + k], pn_[k], im); }
          UNR for (int q = 0; q < 4; q++) { float r[4], i[4];
            UNR for (int e = 0; e < 4; e++) { const unsigned w = LDS1(wb[nu] + (e + 8 * q) * 2048); const float xr = re[4 * q + e], xi = im[4 * q + e];
              r[e] = __builtin_fmaf(xr, bf_lo(w), -(xi * bf_hi(w))); i[e] = __builtin_fmaf(-xr, bf_hi(w), -(xi * bf_lo(w))); }
            UNR for (int e = 0; e < 2; e++) { yr[nu * 2 + (q >> 1)][(q & 1) * 2 + e] = pk_bf16(r[2 * e], r[2 * e + 1]); yi[nu * 2 + (q >> 1)][(q & 1) * 2 + e] = pk_bf16(i[2 * e], i[2 * e + 1]); } } }
      }
      SCHED;
      UNR for (int k = 0; k < 4; k++) yn[k] = yr[k] ^ 0x80008000u;
      u32x4 up[8];
      UNR for (int mu = 0; mu < 2; mu++) { f32x16 re = ZERO16, im = ZERO16;
        UNR for (int k = 0; k < 4; k++) { re = MF32(yr[k], CC[mu * 4 + k], re); im = MF32(yi[k], CC[mu * 4 + k], im);
          re = MF32(yi[k], SS[mu * 4 + k], re); im = MF32(yn[k], SS[mu * 4 + k], im); }
        UNR for (int q = 0; q < 4; q++) { const u32x4 tw = LDS4(ex + (L_TW + mu * 8192 + q * 1024));
          UNR for (int e = 0; e < 4; e++) { float r, i; cmul(re[4 * q + e], im[4 * q + e], tw[e], r, i); up[mu * 4 + q][e] = pk_bf16(r, i); } } }
      SCHED;
      { u32x4 fri[8];
        UNR for (int i = 0; i < 8; i++) fri[i] = LDS4(ex + (L_FR + i * 1024));
        UNR for (int mu = 0; mu < 2; mu++) { f32x16 re = ZERO16, im = ZERO16;
          UNR for (int k = 0; k < 4; k++) { re = MF32(fri[k], up[mu * 4 + k], re); im = MF32(fri[4 + k], up[mu * 4 + k], im); }
          UNR for (int i = 0; i < 16; i++) { xc[mu * 4 + (i >> 2)][i & 3] = xo[mu * 4 + (i >> 2)][i & 3]; xo[mu * 4 + (i >> 2)][i & 3] = pk_bf16(re[i], im[i]); } } }
    }
    STAMP(4);
    BAR;
    {
    LANES;
    { XBASES;
      UNR for (int i = 0; i < 32; i++) { const int o = (i >> 4) * 2048 + ((i >> 2) & 1) * 32768 + (i & 3) * 4096;
        LDS1(xb[(i >> 3) & 1] + o) = xc[i >> 2][i & 3]; LDS1(xb[2 + ((i >> 3) & 1)] + o) = xo[i >> 2][i & 3]; } }
    WAIT_L(0); BAR;
    UNR for (int i = 0; i < 16; i++) ST_SC1(LDS4(wid * 16384 + i * 1024 + lane * 16), OFF_Z + g * SLAB + wid * 16384 + i * 1024 + lane * 16);
    u32x4 g1[4], tw3[4], frr[4], xr_[16];
    const int row0 = g * 64 + wid * 8;
    { const int a = (l31 & 19) | ((l31 & 4) << 1) | ((l31 & 8) >> 1);
      UNR for (int u = 0; u < 2; u++) UNR for (int e = 0; e < 4; e++) { const float t = (float)(a * (8 * u + 4 * hh + (e ^ (wid & 3)))) * (1.0f / 32);
        const float cv = cosr(t), sv = cosr(t - 0.25f); g1[u][e] = pk_bf16(cv, sv); g1[2 + u][e] = pk_bf16(-sv, cv); }
      UNR for (int i = 0; i < 16; i++) { const float t = (float)(a * ((i & 3) + 8 * (i >> 2) + 4 * hh)) * (1.0f / 1024); tw3[i >> 2][i & 3] = pk_f16(cosr(t), cosr(t + 0.25f)); }
      UNR for (int i = 0; i < 4; i++) frr[i] = LDS4(L_FR + i * 1024 + lane * 16); }
    WAIT_V(0); BAR;
    if (tid == 0) FLAG_ST(OFF_F2 + g * 4);
    STAMP(5);
    UNR for (int i = 0; i < 16; i++) xr_[i] = *(const u32x4*)(X + (row0 + (i >> 1)) * 2048 + l31 * 64 + hh * 16 + (i & 1) * 32);
    G2_PRELOAD;
    SCHED;
    if (wid == 0) POLL1(OFF_F2 + l31 * 4);
    WAIT_ALL;
    BAR;
    STAMP(6);
    u32x4 zz[16];
    UNR for (int i = 0; i < 16; i++) zz[i] = LD_SC1(OFF_Z + ((l31 >> 2) * 4 + hh + 2 * (i & 1)) * SLAB + (row0 + (i >> 1)) * 64 + (((lane & 3) ^ ((i >> 2) & 3)) * 16));
    SCHED;
    const float sgn = hh ? 0.f : ((lane & 1) ? 1.f : -1.f);
    UNR for (int r = 0; r < 8; r++) {
      f32x16 re = MF32(zz[2 * r], g1[0], ZERO16), im = MF32(zz[2 * r], g1[2], ZERO16);
      re = MF32(zz[2 * r + 1], g1[1], re); im = MF32(zz[2 * r + 1], g1[3], im);
      const u32x4 z0 = zz[2 * r];
      const unsigned zq = __builtin_amdgcn_readlane((wid & 2) ? ((wid & 1) ? z0[3] : z0[2]) : ((wid & 1) ? z0[1] : z0[0]), 0);
      re[0] = __builtin_fmaf(sgn, bf_hi(zq), re[0]);
      u32x4 up[4];
      UNR for (int i = 0; i < 16; i++) { float a, c; cmul(re[i], im[i], tw3[i >> 2][i & 3], a, c); up[i >> 2][i & 3] = pk_bf16(a, c); }
      f32x16 y = ZERO16;
      UNR for (int k = 0; k < 4; k++) y = MF32(up[k], frr[k], y);
      u32x4 o[2];
      UNR for (int i = 0; i < 8; i++) { const unsigned xv = xr_[2 * r + (i >> 2)][i & 3];
        o[i >> 2][i & 3] = pk_bf16(__builtin_fmaf(y[2 * i], 0x1p-20f, bf_lo(xv)), __builtin_fmaf(y[2 * i + 1], 0x1p-20f, bf_hi(xv))); }
      UNR for (int e = 0; e < 4; e++) { auto sw = __builtin_amdgcn_permlane16_swap(o[0][e], o[1][e], false, false); o[0][e] = sw[0]; o[1][e] = sw[1]; }
      const int to = OFF_T + (row0 + r) * 2048 + fr * 64 + hh * 16 + (fq & 1) * 32;
      ST_SC1(o[0], to); ST_SC1(o[1], to + 1024);
    }
    STAMP(7);
    }
  }

  WAIT_V(0); BAR;
  {
    LANES;
    if (tid == 0) FLAG_ST(isG ? OFF_FH + (pm * 32 + pn) * 4 : OFF_F3 + g * 4);
    STAMP(8);
    const int kg = wid >> 2, rg = wid & 3;
    if (wid == 0) { const int np = nkt >> 1; POLL1(OFF_FH + ((tm >> 1) * 32 + (kt0 >> 1) + (lane < np ? lane : 0)) * 4); }
    G2_VOFF;
    const int va0 = OFF_H + ((tm * 8 + rg * 2 + kg) * 112 + kt0 * 2 + kg) * 1024 + lane * 16, va1 = OFF_H + ((tm * 8 + rg * 2 + (kg ^ 1)) * 112 + kt0 * 2 + kg) * 1024 + lane * 16;
    int bo[8];
    { int q = fr >> 2, p = lane & 3; int row = 32 * kg + 4 * fq + q; int base = row * 256 + (((p >> 1) ^ ((row & 7) << 1)) * 16) + 8 * (p & 1);
      UNR for (int n = 0; n < 8; n++) bo[n] = base ^ (n * 32); }
    const int row = tm * 128 + (rg * 2 + kg) * 16 + fr, colo = tn * 128 + (fq & 1) * 16 + (fq >> 1) * 8;
    const int po = OFF_P + tile * 32768 + wid * 4096 + lane * 16;
    const int eo = half ? OFF_T + row * (D_ * 2) + colo * 2 : po, es = half ? 64 : 1024;
    f32x4 acc[2][8];
    UNR for (int a = 0; a < 2; a++) UNR for (int n = 0; n < 8; n++) acc[a][n] = (f32x4){0, 0, 0, 0};
    u32x4 Ar[4][2], ev[4]; u32x2 B0[8][2], B1[8][2];
    UNR for (int n = 0; n < 8; n++) { TRR(B0[n][0], bo[n], 0); TRR(B0[n][1], bo[n], 4096); }
    BAR;
    STAMP(9);
#define LDH1(sl, t, i) asm volatile("buffer_load_dwordx4 %0, %1, %2, %3 offen sc1" : "=v"(Ar[sl][i]) : "v"((i) ? va1 : va0), "s"(rsW), "s"((t) * 2048) : "memory")
#define LDE(u) asm volatile("buffer_load_dwordx4 %0, %1, %2, %3 offen sc1" : "=v"(ev[u]) : "v"(eo), "s"(rsW), "s"((u) * es) : "memory")
#define WAIT_LD(B) asm volatile("s_waitcnt lgkmcnt(0)" : "+v"(B[0][0]), "+v"(B[0][1]), "+v"(B[1][0]), "+v"(B[1][1]), "+v"(B[2][0]), "+v"(B[2][1]), "+v"(B[3][0]), "+v"(B[3][1]), \
   "+v"(B[4][0]), "+v"(B[4][1]), "+v"(B[5][0]), "+v"(B[5][1]), "+v"(B[6][0]), "+v"(B[6][1]), "+v"(B[7][0]), "+v"(B[7][1])::"memory")
#define WAIT_VA(n, sl) asm volatile("s_waitcnt vmcnt(" #n ")" : "+v"(Ar[sl][0]), "+v"(Ar[sl][1])::"memory")
#define FLIP do { UNR for (int n = 0; n < 8; n++) bo[n] ^= 65536; } while (0)
#define ITER(t, Bc, Bn, sl, so, VC, ISB, ISA, RD, DOBAR) do { WAIT_VA(VC, sl); WAIT_LD(Bc); if (DOBAR) BAR; SCHED; \
    UNR for (int n = 0; n < 8; n++) { \
      if (RD) { TRR(Bn[n][0], bo[n], so); TRR(Bn[n][1], bo[n], so + 4096); } \
      acc[n & 1][n] = MF16(CAT(Bc[n]), Ar[sl][n & 1], acc[n & 1][n]); SCHED; \
      acc[(n & 1) ^ 1][n] = MF16(CAT(Bc[n]), Ar[sl][(n & 1) ^ 1], acc[(n & 1) ^ 1][n]); SCHED; \
      if (ISB && n == 0) STD1((t) + 7, 0); if (ISB && n == 2) STD1((t) + 7, 1); \
      if (ISA == 1 && n == 4) LDH1(((sl) + 3) & 3, (t) + 3, 0); if (ISA == 1 && n == 6) LDH1(((sl) + 3) & 3, (t) + 3, 1); \
      if (ISA == 2 && n < 4) LDE(n); SCHED; } } while (0)
    LDH1(0, 0, 0); LDH1(0, 0, 1); LDH1(1, 1, 0); LDH1(1, 1, 1); LDH1(2, 2, 0); LDH1(2, 2, 1);
    ITER(0, B0, B1, 0, 16384, 4, 0, 1, 1, 1); ITER(1, B1, B0, 1, 32768, 4, 1, 1, 1, 0);
    ITER(2, B0, B1, 2, 49152, 6, 1, 1, 1, 1); FLIP; ITER(3, B1, B0, 3, 0, 8, 1, 1, 1, 0);
    int t;
    for (t = 4; t < nkt - 8; t += 4) {
      ITER(t, B0, B1, 0, 16384, 8, 1, 1, 1, 1); ITER(t + 1, B1, B0, 1, 32768, 8, 1, 1, 1, 0);
      ITER(t + 2, B0, B1, 2, 49152, 8, 1, 1, 1, 1); FLIP; ITER(t + 3, B1, B0, 3, 0, 8, 1, 1, 1, 0); }
    ITER(t, B0, B1, 0, 16384, 8, 1, 1, 1, 1); ITER(t + 1, B1, B0, 1, 32768, 8, 0, 1, 1, 0);
    ITER(t + 2, B0, B1, 2, 49152, 6, 0, 1, 1, 1); FLIP; ITER(t + 3, B1, B0, 3, 0, 4, 0, 1, 1, 0);
    ITER(t + 4, B0, B1, 0, 16384, 4, 0, 1, 1, 1); ITER(t + 5, B1, B0, 1, 32768, 4, 0, 0, 1, 0);
    if (wid == 0) POLL1(half ? OFF_F3 + (2 * tm + (lane & 1)) * 4 : OFF_FP + tile * 128);
    ITER(t + 6, B0, B1, 2, 49152, 2, 0, 2, 1, 1); ITER(t + 7, B1, B0, 3, 0, 4, 0, 0, 0, 1);
    STAMP(10);
    UNR for (int n = 0; n < 8; n++) *(LAS f32x4*)(lds + wid * 8192 + n * 1024 + lane * 16) = acc[1][n];
    WAIT_L(0); BAR;
    UNR for (int n = 0; n < 8; n++) acc[0][n] += *(LAS f32x4*)(lds + (wid ^ 4) * 8192 + n * 1024 + lane * 16);
    UNR for (int u = 0; u < 4; u++) UNR for (int j = 0; j < 4; j++) {
      auto sw = __builtin_amdgcn_permlane16_swap(__float_as_uint(acc[0][2 * u][j]), __float_as_uint(acc[0][2 * u + 1][j]), false, false);
      acc[0][2 * u][j] = __uint_as_float(sw[0]); acc[0][2 * u + 1][j] = __uint_as_float(sw[1]); }
    asm volatile("s_waitcnt vmcnt(0)" : "+v"(ev[0]), "+v"(ev[1]), "+v"(ev[2]), "+v"(ev[3])::"memory");
    UNR for (int u = 0; u < 4; u++) { const f32x4 lo = acc[0][2 * u], hi = acc[0][2 * u + 1];
      const u32x4 w = {pk_bf16(lo[0] + bf_lo(ev[u][0]), lo[1] + bf_hi(ev[u][0])), pk_bf16(lo[2] + bf_lo(ev[u][1]), lo[3] + bf_hi(ev[u][1])),
                       pk_bf16(hi[0] + bf_lo(ev[u][2]), hi[1] + bf_hi(ev[u][2])), pk_bf16(hi[2] + bf_lo(ev[u][3]), hi[3] + bf_hi(ev[u][3]))};
      if (half) ST_SC1(w, po + u * 1024); else *(u32x4*)(O + row * (D_ * 2) + (colo + 32 * u) * 2) = w; }
    if (half) { WAIT_V(0); BAR; if (tid == 0) FLAG_ST(OFF_FP + tile * 128); }
    STAMP(11);
  }
}

extern "C" void kernel_launch(void* const* d_in, const int* in_sizes, int n_in, void* d_out, int out_size, void* d_ws, size_t ws_size, hipStream_t stream) {
  (void)in_sizes; (void)n_in; (void)out_size; (void)ws_size;
  (void)hipFuncSetAttribute((const void*)fused_k, hipFuncAttributeMaxDynamicSharedMemorySize, LDS_BYTES);
  fused_k<<<256, 512, LDS_BYTES, stream>>>((const char*)d_in[0], (const char*)d_in[1], (const char*)d_in[2], (const char*)d_in[4],
                                           (const char*)d_in[5], (const char*)d_in[6], (char*)d_out, (char*)d_ws);
}
```

```cpp
#include <hip/hip_runtime.h>
#include <stdint.h>

#define LAS __attribute__((address_space(3)))
#define GAS __attribute__((address_space(1)))
typedef short bf16x8 __attribute__((ext_vector_type(8)));
typedef float f32x4 __attribute__((ext_vector_type(4)));
typedef float f32x16 __attribute__((ext_vector_type(16)));
typedef unsigned u32x4 __attribute__((ext_vector_type(4)));
typedef unsigned u32x2 __attribute__((ext_vector_type(2)));
typedef u32x2 u32x2u __attribute__((aligned(2)));
typedef __bf16 bf16x2v __attribute__((ext_vector_type(2)));
typedef _Float16 h16x2 __attribute__((ext_vector_type(2)));
typedef float f32x2 __attribute__((ext_vector_type(2)));

constexpr int S_ = 2048, D_ = 1024, F_ = 3584;
constexpr int OFF_E = 0, OFF_F2 = 4096, OFF_FP = 16384, OFF_R = 32768, OFF_FF = 65536, OFF_CS = 131072, OFF_F1 = 655360, OFF_F3 = OFF_F1 + 32768, OFF_FH = OFF_F3 + 32768;
constexpr int SLAB = 135424;
constexpr int OFF_X1 = 1 << 20, OFF_WT = 6 << 20, OFF_Z = 11 << 20, OFF_T = 16 << 20, OFF_P = 20 << 20, OFF_H = 24 << 20, WS_USED = 40 << 20;
constexpr int L_EX = 131072, L_T1 = L_EX + 8192, L_G1 = L_EX + 12288, L_FR = L_EX + 16384, L_SC = L_EX + 24576, L_TW = L_EX, LDS_BYTES = 163840;
constexpr int NA = 32;
constexpr unsigned SPIN_CAP = 1u << 18;

__device__ __forceinline__ unsigned pk_bf16(float lo, float hi) { f32x2 v = {lo, hi}; return __builtin_bit_cast(unsigned, __builtin_convertvector(v, bf16x2v)); }
__device__ __forceinline__ unsigned pk_f16(float lo, float hi) { h16x2 v = {(_Float16)lo, (_Float16)hi}; return __builtin_bit_cast(unsigned, v); }
__device__ __forceinline__ float bf_lo(unsigned u) { return __uint_as_float(u << 16); }
__device__ __forceinline__ float bf_hi(unsigned u) { return __uint_as_float(u & 0xffff0000u); }
__device__ __forceinline__ float rbf(float f) { return bf_lo(pk_bf16(f, f)); }
__device__ __forceinline__ float cosr(float rev) { return __builtin_amdgcn_cosf(rev); }
__device__ __forceinline__ float dot2(unsigned a, unsigned b, float c) { return __builtin_amdgcn_fdot2_f32_bf16(__builtin_bit_cast(bf16x2v, a), __builtin_bit_cast(bf16x2v, b), c, false); }
__device__ __forceinline__ void cmul(float re, float im, unsigned t, float& r2, float& i2) {
  h16x2 tw = __builtin_bit_cast(h16x2, t);
  float a = __builtin_fmaf(im, (float)tw[1], 0.0f), b = __builtin_fmaf(im, (float)tw[0], 0.0f);
  r2 = __builtin_fmaf(re, (float)tw[0], -a); i2 = __builtin_fmaf(re, (float)tw[1], b);
  asm("" : "+v"(r2)); asm("" : "+v"(i2));
}
template <int CTRL> __device__ __forceinline__ float dppf(float v) { return __builtin_bit_cast(float, __builtin_amdgcn_update_dpp(0, __builtin_bit_cast(int, v), CTRL, 0xf, 0xf, true)); }
__device__ __forceinline__ float par_sum(float v) {
  v += dppf<0x4E>(v); v += dppf<0x124>(v); v += dppf<0x128>(v);
  auto a = __builtin_amdgcn_permlane16_swap(__float_as_uint(v), __float_as_uint(v), false, false); v = __uint_as_float(a[0]) + __uint_as_float(a[1]);
  auto b = __builtin_amdgcn_permlane32_swap(__float_as_uint(v), __float_as_uint(v), false, false); return __uint_as_float(b[0]) + __uint_as_float(b[1]);
}
__device__ __forceinline__ float rdl(float v, int l) { return __builtin_bit_cast(float, __builtin_amdgcn_readlane(__builtin_bit_cast(int, v), l)); }

__device__ __forceinline__ int opq(int v) { asm volatile("" : "+v"(v)); return v; }
__device__ __forceinline__ int lane_id() { int v; asm volatile("v_mbcnt_lo_u32_b32 %0, -1, 0\n\tv_mbcnt_hi_u32_b32 %0, -1, %0" : "=v"(v)); __builtin_assume(v >= 0 && v < 64); return v; }
#define LANES const int lane = lane_id(), tid = wid * 64 + lane, l31 = lane & 31, hh = lane >> 5, fr = lane & 15, fq = lane >> 4; (void)tid; (void)l31; (void)hh; (void)fr; (void)fq
#define BC8(x) __builtin_bit_cast(bf16x8, x)
#define MF32(a, b, c) __builtin_amdgcn_mfma_f32_32x32x16_bf16(BC8(a), BC8(b), c, 0, 0, 0)
#define MF16(a, b, c) __builtin_amdgcn_mfma_f32_16x16x32_bf16(BC8(a), BC8(b), c, 0, 0, 0)
#define WAIT_V(n) asm volatile("s_waitcnt vmcnt(" #n ")" ::: "memory")
#define WAIT_L(n) asm volatile("s_waitcnt lgkmcnt(" #n ")" ::: "memory")
#define WAIT_ALL do { __builtin_amdgcn_s_waitcnt(0x0070); asm volatile("" ::: "memory"); } while (0)
#define BAR __builtin_amdgcn_s_barrier()
#define SCHED __builtin_amdgcn_sched_barrier(0)
#define UNR _Pragma("unroll")
#define LDS4(off) (*(LAS u32x4*)(lds + (off)))
#define LDS1(off) (*(LAS unsigned*)(lds + (off)))
#define ST_SC1(v, off) __builtin_amdgcn_raw_buffer_store_b128(v, rsW, off, 0, 16)
#define LD_SC1(off) __builtin_amdgcn_raw_buffer_load_b128(rsW, off, 0, 16)
#define FLAG_ST(off) __hip_atomic_store((GAS unsigned*)(ws + (off)), epoch, __ATOMIC_RELAXED, __HIP_MEMORY_SCOPE_AGENT)
#define FLAG_LD(off) __hip_atomic_load((GAS unsigned*)(ws + (off)), __ATOMIC_RELAXED, __HIP_MEMORY_SCOPE_AGENT)
#define POLLS(off, slp) do { const int po_ = (off); _Pragma("unroll 1") for (unsigned sp_ = 0; sp_ < SPIN_CAP && FLAG_LD(po_) != epoch; ++sp_) __builtin_amdgcn_s_sleep(slp); \
    __builtin_amdgcn_fence(__ATOMIC_ACQUIRE, "wavefront"); } while (0)
#define POLL1(off) POLLS(off, 1)
#define TRR(dst, base, off) asm volatile("ds_read_b64_tr_b16 %0, %1 offset:%2" : "=v"(dst) : "v"(base), "i"(off) : "memory")
#define CAT(x) __builtin_shufflevector(x[0], x[1], 0, 1, 2, 3)
#define STAMP(i)
#define XSTAMP(i)
#define XPUT(i)
static const f32x16 ZERO16 = {0, 0, 0, 0, 0, 0, 0, 0, 0, 0, 0, 0, 0, 0, 0, 0};

__global__ __launch_bounds__(512, 2) void fused_k(const char* __restrict__ X, const char* __restrict__ Wr, const char* __restrict__ Wi,
                                                  const char* __restrict__ Wg, const char* __restrict__ Wu, const char* __restrict__ Wd,
                                                  char* __restrict__ O, char* ws) {
  extern __shared__ __attribute__((aligned(16))) char smem_[];
  LAS char* lds = (LAS char*)smem_;
  const int b = blockIdx.x, xg = b & 7, rr = b >> 3;
  const bool isG = rr < 28;
  const int g = xg * 4 + (rr & 3);
  const int wid = __builtin_amdgcn_readfirstlane(threadIdx.x >> 6);
  const auto rsW = __builtin_amdgcn_make_buffer_rsrc(ws, 0, WS_USED, 0x00020000);
  STAMP(0);

  const int pm = (xg & 1) * 4 + (rr & 3), pn = (xg >> 1) * 7 + (rr >> 2);
  const int wr = wid >> 2, wc = wid & 3, kr = isG ? (xg & 1) * 8 : 0;
  const int g28 = g < 28 ? g : g - 28;
  const char* cA = isG ? X + pm * 256 * (D_ * 2) : Wd + (g >> 3) * 256 * (D_ * 2) + (g & 7) * 256;
  const char* cG = isG ? Wg + pn * 256 : Wd + 384 * (F_ * 2) + g28 * 256;
  const char* cU = isG ? Wu + pn * 256 : Wd + 512 * (F_ * 2) + g28 * 256;
  const unsigned ldsw = wid * 1024;
#define G1_VOFF unsigned voffA, voffB; { int row = wid * 8 + (lane >> 3); voffA = row * (D_ * 2) + (((lane & 7) ^ (lane >> 3)) * 16); } \
  { int row = wid * 4 + fq; int swz = ((row & 3) | ((row >> 1) & 4)) << 1; voffB = row * (F_ * 2) + ((fr ^ swz) * 16); }
#define SA(bf, h) (((bf) * 2 + (h)) * 16384)
#define SB(bf, h) ((4 + (bf) * 2 + (h)) * 16384)
#define STA(bufoff, h, t) do { const char* g_ = cA + (h) * 128 * (D_ * 2) + ((t) ^ kr) * 128; UNR for (int i_ = 0; i_ < 2; ++i_) \
    __builtin_amdgcn_global_load_lds((const unsigned*)(g_ + i_ * 64 * (D_ * 2) + voffA), (LAS unsigned*)(lds + (bufoff) + ldsw + i_ * 8192), 16, 0, 0); } while (0)
#define STB(bufoff, base, t) do { const char* g_ = (base) + ((t) ^ kr) * (64 * F_ * 2); UNR for (int i_ = 0; i_ < 2; ++i_) \
    __builtin_amdgcn_global_load_lds((const unsigned*)(g_ + i_ * 32 * (F_ * 2) + voffB), (LAS unsigned*)(lds + (bufoff) + ldsw + i_ * 8192), 16, 0, 0); } while (0)
  const int s = b * 8 + wid, sq = (wid >> 1) & 3, se = b & 3;
#define GLD(dst, ins, voff, sbase, mod) asm volatile(ins " %0, %1, %2 " mod : "=v"(dst) : "v"(voff), "s"(sbase) : "memory")
#define TAB_T1 do { const float a0 = (float)(2 * (wid & 1) + 8 * (wid >> 1) + 4 * hh), m2 = (float)l31 * (1.0f / 1024), t0 = a0 * m2, t1 = t0 + m2; \
    *(LAS u32x2*)(lds + L_T1 + (wid >> 1) * 1024 + lane * 16 + (wid & 1) * 8) = (u32x2){pk_f16(cosr(t0), cosr(t0 + 0.25f)), pk_f16(cosr(t1), cosr(t1 + 0.25f))}; } while (0)
  unsigned epoch;
  {
  LANES;
  unsigned e0, w5r, w5i; u32x4 xa[2]; u32x2 wrv[2], wiv[2];
  { const int xo_ = l31 * 64 + hh * 16;
    GLD(xa[0], "global_load_dwordx4", xo_, X + s * 2048, ""); GLD(xa[1], "global_load_dwordx4", xo_, X + s * 2048, "offset:32");
    GLD(e0, "global_load_dword", 0, ws + OFF_E, "sc1"); }
  G1_VOFF;
  SCHED;
  STB(SB(0, 0), cG, 0); STA(SA(0, 0), 0, 0); STB(SB(0, 1), cU, 0); STA(SA(0, 1), 1, 0);
  SCHED;
  {
    const int e2 = ((l31 >> 1) & 1) + 2 * ((l31 >> 3) & 1), k2 = 4 * (2 * (l31 >> 4) + ((l31 >> 2) & 1)) + (e2 ^ se);
    const float mult = (float)(wid < 4 ? l31 : k2) * (1.0f / 32);
    const float ph = 0.25f * (float)(wid < 4 ? (wid >> 1) : (l31 & 1) - ((wid >> 1) & 1));
    const float b0 = (float)(16 * (wid & 1) + 4 * hh);
    float v[8];
    UNR for (int e = 0; e < 8; e++) v[e] = cosr(__builtin_fmaf(b0 + (float)((e & 3) + 8 * (e >> 2)), mult, ph));
    LDS4(L_EX + wid * 1024 + lane * 16) = (u32x4){pk_bf16(v[0], v[1]), pk_bf16(v[2], v[3]), pk_bf16(v[4], v[5]), pk_bf16(v[6], v[7])};
    TAB_T1;
  }
  WAIT_L(0); BAR;
  XSTAMP(12);
  { const int wo_ = ((lane >> 2) & 7) * 8 + (lane & 3) * 64 + hh * 256;
    GLD(wrv[0], "global_load_dwordx2", wo_, Wr + s * 1026, ""); GLD(wiv[0], "global_load_dwordx2", wo_, Wi + s * 1026, "");
    GLD(wrv[1], "global_load_dwordx2", wo_, Wr + s * 1026, "offset:512"); GLD(wiv[1], "global_load_dwordx2", wo_, Wi + s * 1026, "offset:512");
    GLD(w5r, "global_load_ushort", 0, Wr + s * 1026, "offset:1024"); GLD(w5i, "global_load_ushort", 0, Wi + s * 1026, "offset:1024"); }
  {
    u32x4 tb[12];
    UNR for (int i = 0; i < 12; i++) tb[i] = LDS4(L_EX + i * 1024 + lane * 16);
    {
      const int a = (l31 & 19) | ((l31 & 4) << 1) | ((l31 & 8) >> 1), sx_ = wid < 4 ? se : 0;
      const float mult = (float)(wid < 4 ? a : l31) * (1.0f / 32), b0 = (float)(8 * (wid < 4 ? wid & 1 : wid & 3) + 4 * hh), ph = wid < 4 ? 0.25f * (float)(wid >> 1) : 0.f;
      u32x4 w;
      UNR for (int e = 0; e < 4; e++) { const float t = __builtin_fmaf(b0 + (float)(e ^ sx_), mult, ph); w[e] = pk_bf16(cosr(t), cosr(t - 0.25f)); }
      LDS4(L_G1 + wid * 1024 + lane * 16) = w; }
    u32x4 idm[2];
    UNR for (int j = 0; j < 2; j++) { const int d = l31 - 16 * j - 8 * hh;
      UNR for (int p = 0; p < 4; p++) idm[j][p] = (d >> 1) == p ? (0x3f80u << (16 * (d & 1))) : 0u; }
    asm volatile("s_waitcnt vmcnt(15)" : "+v"(xa[0]), "+v"(xa[1])::"memory");
    XSTAMP(13);
    f32x16 xt = MF32(xa[0], idm[0], ZERO16); xt = MF32(xa[1], idm[1], xt);
    float s0 = 0.f, s1 = 0.f;
    UNR for (int j = 0; j < 2; j++) UNR for (int p = 0; p < 4; p++) { s0 = dot2(xa[j][p], 0x3f803f80u, s0); s1 = dot2(xa[j][p], 0xbf803f80u, s1); }
    const float sv = par_sum(((lane & 1) ? s1 : s0) + dppf<0xB1>((lane & 1) ? s0 : s1));
    const float mu = rdl(sv, 0) * (1.0f / D_), alt = rdl(sv, 1), m = rbf(mu);
    float q = 0.f;
    UNR for (int i = 0; i < 16; i++) { xt[i] -= m; q = __builtin_fmaf(xt[i], xt[i], q); }
    u32x4 df[2];
    UNR for (int i = 0; i < 8; i++) df[i >> 2][i & 3] = pk_bf16(xt[2 * i], xt[2 * i + 1]);
    f32x16 tr = MF32(df[0], tb[0], ZERO16), ti = MF32(df[0], tb[2], ZERO16);
    tr = MF32(df[1], tb[1], tr); ti = MF32(df[1], tb[3], ti);
    q = par_sum(q + dppf<0xB1>(q));
    const float var = fmaxf(q * (1.0f / D_) - (mu - m) * (mu - m), 0.f);
    const float sd = rbf(__builtin_amdgcn_sqrtf(rbf(rbf(var) + 1e-5f))), rs = __builtin_amdgcn_rcpf(sd);
    SCHED; STB(SB(1, 0), cG, 1); STA(SA(1, 0), 0, 1); STB(SB(1, 1), cU, 1); SCHED;
    u32x4 br[2], bi[2];
    UNR for (int i = 0; i < 8; i++) { float r0, i0, r1, i1;
      cmul(tr[2 * i], ti[2 * i], tb[8 + (i >> 1)][(2 * i) & 3], r0, i0); cmul(tr[2 * i + 1], ti[2 * i + 1], tb[8 + (i >> 1)][(2 * i + 1) & 3], r1, i1);
      br[i >> 2][i & 3] = pk_bf16(r0, r1); bi[i >> 2][i & 3] = pk_bf16(i0, i1); }
    f32x16 xo = MF32(tb[4], br[0], ZERO16); xo = MF32(tb[6], bi[0], xo); xo = MF32(tb[5], br[1], xo); xo = MF32(tb[7], bi[1], xo);
    UNR for (int e = 0; e < 4; e++) xo[2 * e + 1] = (lane == 0 && se == e) ? alt : xo[2 * e + 1];
    asm volatile("s_waitcnt vmcnt(6)" : "+v"(e0), "+v"(wrv[0]), "+v"(wrv[1]), "+v"(wiv[0]), "+v"(wiv[1]), "+v"(w5r), "+v"(w5i)::"memory");
    epoch = __builtin_amdgcn_readfirstlane(e0) * 0x9E3779B1u + 0x7F4A7C15u;
    const int xoff = OFF_X1 + ((l31 >> 2) * 4 + hh) * SLAB + s * 64 + (((lane & 3) ^ sq) * 16);
    UNR for (int u = 0; u < 2; u++) { u32x4 w;
      UNR for (int e = 0; e < 4; e++) w[e] = pk_bf16(xo[8 * u + 2 * e] * rs, xo[8 * u + 2 * e + 1] * rs);
      ST_SC1(w, xoff + 2 * u * SLAB); }
    const unsigned sel0 = (se & 1) ? 0x07060302u : 0x05040100u, sel1 = sel0 ^ 0x02020202u;
    UNR for (int u = 0; u < 2; u++) { const unsigned ra = (se & 2) ? wrv[u][1] : wrv[u][0], rb = (se & 2) ? wrv[u][0] : wrv[u][1];
      const unsigned ia = (se & 2) ? wiv[u][1] : wiv[u][0], ib = (se & 2) ? wiv[u][0] : wiv[u][1];
      u32x4 w = {__builtin_amdgcn_perm(ia, ra, sel0), __builtin_amdgcn_perm(ia, ra, sel1), __builtin_amdgcn_perm(ib, rb, sel0), __builtin_amdgcn_perm(ib, rb, sel1)};
      ST_SC1(w, xoff + (OFF_WT - OFF_X1) + 2 * u * SLAB); }
    u32x4 rec = {pk_bf16(m, sd), epoch, w5r | (w5i << 16), __builtin_amdgcn_perm(wiv[0][0], wrv[0][0], 0x05040100u)};
    ST_SC1(rec, lane == 0 ? OFF_R + s * 16 : WS_USED);
    XPUT(12); XPUT(13);
  }
  }
  SCHED;
  STAMP(1);

  const int half = xg & 1, tm = (xg >> 1) * 4 + (rr & 3), tn = rr >> 2, tile = tm * 8 + tn;
  const int kt0 = half ? NA : 0, nkt = half ? 56 - NA : NA;
  const char* cB = Wd + (size_t)kt0 * 64 * (D_ * 2) + tn * 256;
#define G2_VOFF unsigned voffD; { int row = wid * 4 + fq; voffD = row * (D_ * 2) + ((fr ^ ((row & 7) << 1)) * 16); }
#define STD1(t, i) __builtin_amdgcn_global_load_lds((const unsigned*)(cB + (size_t)(t) * 64 * (D_ * 2) + (i) * 32 * (D_ * 2) + voffD), (LAS unsigned*)(lds + (((t) & 7) * 16384) + ldsw + (i) * 8192), 16, 0, 0)
#define G2_PRELOAD do { G2_VOFF; UNR for (int t_ = 0; t_ < 8; t_++) { STD1(t_, 0); STD1(t_, 1); } } while (0)

#define BLD(dst, ins, voff, mod) asm volatile(ins " %0, %1, %2, 0 offen " mod : "=v"(dst) : "v"(voff), "s"(rsW) : "memory")
#define P3_LOAD do { const int zo_ = OFF_Z + ((l31 >> 2) * 4 + hh) * SLAB + s * 64 + (((lane & 3) ^ sq) * 16), xo_ = l31 * 64 + hh * 16; \
    BLD(zz[0], "buffer_load_dwordx4", zo_, "sc1"); BLD(zz[1], "buffer_load_dwordx4", zo_ + 2 * SLAB, "sc1"); \
    GLD(zz[2], "global_load_dwordx4", xo_, X + s * 2048, ""); GLD(zz[3], "global_load_dwordx4", xo_, X + s * 2048, "offset:32"); } while (0)
#define P3_RUN(VC) do { u32x4 g1[4], tw3[4], frr[4]; \
    { const int ex = opq(L_EX + lane * 16) - L_EX, ea = opq(L_EX + ((lane & 51) | ((lane & 4) << 1) | ((lane & 8) >> 1)) * 16) - L_EX; \
      UNR for (int i = 0; i < 4; i++) { g1[i] = LDS4(ex + (L_G1 + i * 1024)); tw3[i] = LDS4(ea + (L_T1 + i * 1024)); frr[i] = LDS4(ex + (L_FR + i * 1024)); } } \
    const float sgn = hh ? 0.f : ((lane & 1) ? 1.f : -1.f); \
    asm volatile("s_waitcnt vmcnt(" #VC ")" : "+v"(zz[0]), "+v"(zz[1]), "+v"(zz[2]), "+v"(zz[3])::"memory"); \
    f32x16 re = MF32(zz[0], g1[0], ZERO16), im = MF32(zz[0], g1[2], ZERO16); \
    re = MF32(zz[1], g1[1], re); im = MF32(zz[1], g1[3], im); \
    const unsigned zq = __builtin_amdgcn_readlane((se & 2) ? ((se & 1) ? zz[0][3] : zz[0][2]) : ((se & 1) ? zz[0][1] : zz[0][0]), 0); \
    re[0] = __builtin_fmaf(sgn, bf_hi(zq), re[0]); \
    u32x4 up[4]; \
    UNR for (int i = 0; i < 16; i++) { float a_, c_; cmul(re[i], im[i], tw3[i >> 2][i & 3], a_, c_); up[i >> 2][i & 3] = pk_bf16(a_, c_); } \
    f32x16 y = ZERO16; \
    UNR for (int k = 0; k < 4; k++) y = MF32(up[k], frr[k], y); \
    u32x4 o[2]; \
    UNR for (int i = 0; i < 8; i++) { const unsigned xv = zz[2 + (i >> 2)][i & 3]; \
      o[i >> 2][i & 3] = pk_bf16(__builtin_fmaf(y[2 * i], 0x1p-20f, bf_lo(xv)), __builtin_fmaf(y[2 * i + 1], 0x1p-20f, bf_hi(xv))); } \
    UNR for (int e = 0; e < 4; e++) { auto sw = __builtin_amdgcn_permlane16_swap(o[0][e], o[1][e], false, false); o[0][e] = sw[0]; o[1][e] = sw[1]; } \
    const int to = OFF_T + s * 2048 + fr * 64 + hh * 16 + (fq & 1) * 32; \
    ST_SC1(o[0], to); ST_SC1(o[1], to + 1024); } while (0)
  bool p3_done = false;

  if (isG) {
    LANES; G1_VOFF;
    const int aoff0 = (wr * 64 + fr) * 128 + ((fq ^ (fr & 7)) * 16), aoff1 = aoff0 ^ 64;
    int bo0, bo1;
    { int q = fr >> 2, p = lane & 3; int row = 8 * fq + q; int swz = ((row & 3) | ((row >> 1) & 4)) << 1;
      bo0 = row * 256 + (((4 * wc + (p >> 1)) ^ swz) * 16) + 8 * (p & 1) + 65536; bo1 = bo0 ^ 32; }
#define LDA(dst, bf, h) do { UNR for (int m = 0; m < 4; ++m) { dst[m][0] = LDS4(SA(bf, h) + aoff0 + m * 2048); dst[m][1] = LDS4(SA(bf, h) + aoff1 + m * 2048); } } while (0)
#define LDB(dst, bf, h) do { TRR(dst[0][0][0], bo0, SB(bf, h) - 65536); TRR(dst[0][0][1], bo0, SB(bf, h) - 65536 + 1024); \
    TRR(dst[1][0][0], bo1, SB(bf, h) - 65536); TRR(dst[1][0][1], bo1, SB(bf, h) - 65536 + 1024); \
    TRR(dst[0][1][0], bo0, SB(bf, h) - 65536 + 8192); TRR(dst[0][1][1], bo0, SB(bf, h) - 65536 + 9216); \
    TRR(dst[1][1][0], bo1, SB(bf, h) - 65536 + 8192); TRR(dst[1][1][1], bo1, SB(bf, h) - 65536 + 9216); } while (0)
#define WAIT_LB(B) asm volatile("s_waitcnt lgkmcnt(0)" : "+v"(B[0][0][0]), "+v"(B[0][0][1]), "+v"(B[0][1][0]), "+v"(B[0][1][1]), "+v"(B[1][0][0]), "+v"(B[1][0][1]), "+v"(B[1][1][0]), "+v"(B[1][1][1])::"memory")
#define MMA(ai, bj, Bt) do { __builtin_amdgcn_s_setprio(1); UNR for (int i = 0; i < 16; ++i) { const int m = i >> 2, n = ((i >> 1) ^ (i >> 2)) & 1, k = (i ^ (i >> 1)) & 1; \
    acc[ai][bj][m][n] = MF16(CAT(Bt[n][k]), At[m][k], acc[ai][bj][m][n]); SCHED; } __builtin_amdgcn_s_setprio(0); } while (0)
#define CSUM(on, Bt) do { if (on) { UNR for (int i = 0; i < 16; i++) cs[i & 1] = dot2(Bt[i & 1][(i >> 1) & 1][(i >> 2) & 1][i >> 3], 0x3f803f80u, cs[i & 1]); } SCHED; } while (0)
    f32x4 acc[2][2][4][2];
    UNR for (int a = 0; a < 2; a++) UNR for (int c = 0; c < 2; c++) UNR for (int m = 0; m < 4; m++) UNR for (int n = 0; n < 2; n++) acc[a][c][m][n] = (f32x4){0, 0, 0, 0};
    float cs[2] = {0.f, 0.f};
    const int tc = (rr & 3) * 2, tc0 = wr ? -1 : tc, tc1 = wr ? tc : -1;
    u32x4 At[4][2]; u32x2 B0[2][2][2], B1[2][2][2];
    BAR;
    if (wr == 1) BAR;
#define KTILE(bf, t, tp) do { \
    LDB(B0, bf, 0); SCHED; LDA(At, bf, 0); STA(SA(1 - bf, 1), 1, (t) + 1); \
    WAIT_L(8); BAR; WAIT_LB(B0); MMA(0, 0, B0); BAR; SCHED; \
    LDB(B1, bf, 1); STB(SB(bf, 0), cG, (t) + 2); CSUM(tp == tc0, B0); \
    BAR; WAIT_LB(B1); MMA(0, 1, B1); BAR; \
    LDA(At, bf, 1); STA(SA(bf, 0), 0, (t) + 2); \
    BAR; WAIT_L(0); MMA(1, 0, B0); BAR; SCHED; \
    STB(SB(bf, 1), cU, (t) + 2); CSUM(tp == tc1, B1); \
    WAIT_V(6); BAR; MMA(1, 1, B1); BAR; } while (0)
    for (int t = 0; t < 14; t += 2) {
      KTILE(0, t, t);
      STAMP(16 + t);
      if (t == 0 && tid == 0) FLAG_ST(OFF_F1 + b * 128);
      KTILE(1, t + 1, t);
      STAMP(17 + t);
      if (t == tc) {
        UNR for (int n = 0; n < 2; n++) { float v = cs[n];
          auto a = __builtin_amdgcn_permlane16_swap(__float_as_uint(v), __float_as_uint(v), false, false); v = __uint_as_float(a[0]) + __uint_as_float(a[1]);
          auto d = __builtin_amdgcn_permlane32_swap(__float_as_uint(v), __float_as_uint(v), false, false); cs[n] = __uint_as_float(d[0]) + __uint_as_float(d[1]); }
        const int l_ = lane_id();
        __builtin_amdgcn_raw_buffer_store_b64((u32x2){__float_as_uint((l_ & 16) ? cs[1] : cs[0]), epoch}, rsW, l_ < 32 ? OFF_CS + pn * 16384 + pm * 2048 + wid * 256 + l_ * 8 : WS_USED, 0, 16);
        SCHED; }
    }
    u32x2 rec, cq[4];
    const int ro = OFF_R + (pm * 256 + (tid & 255)) * 16, co = OFF_CS + pn * 16384 + wr * 8192 + (tid & 255) * 8;
#define EPI_LOAD do { BLD(rec, "buffer_load_dwordx2", ro, "sc1"); BLD(cq[0], "buffer_load_dwordx2", co, "sc1"); BLD(cq[1], "buffer_load_dwordx2", co, "offset:2048 sc1"); \
    BLD(cq[2], "buffer_load_dwordx2", co + 4096, "sc1"); BLD(cq[3], "buffer_load_dwordx2", co + 4096, "offset:2048 sc1"); } while (0)
#define EPI_WAIT asm volatile("s_waitcnt vmcnt(0)" : "+v"(rec), "+v"(cq[0]), "+v"(cq[1]), "+v"(cq[2]), "+v"(cq[3])::"memory")
    LDB(B0, 0, 0); LDA(At, 0, 0); STA(SA(1, 1), 1, 15); EPI_LOAD;
    BAR; WAIT_LB(B0); MMA(0, 0, B0); BAR;
    LDB(B1, 0, 1); BAR; WAIT_LB(B1); MMA(0, 1, B1); BAR;
    LDA(At, 0, 1); WAIT_V(9); BAR; WAIT_L(0); MMA(1, 0, B0); MMA(1, 1, B1); BAR;
    LDB(B0, 1, 0); LDA(At, 1, 0); WAIT_V(7); BAR; WAIT_LB(B0); MMA(0, 0, B0); BAR;
    LDB(B1, 1, 1); EPI_WAIT;
    for (unsigned sp = 0; sp < SPIN_CAP && !__all(rec[1] == epoch && cq[0][1] == epoch && cq[1][1] == epoch && cq[2][1] == epoch && cq[3][1] == epoch); ++sp) {
      __builtin_amdgcn_s_sleep(1); EPI_LOAD; EPI_WAIT; }
    unsigned f2;
    BLD(f2, "buffer_load_dword", wid == 0 ? OFF_F2 + rr * 128 + l31 * 4 : WS_USED, "sc1");
    { const float m = bf_lo(rec[0]), sd = bf_hi(rec[0]), r = __builtin_amdgcn_rcpf(sd);
      const float c0 = -2.302208198f;
      *(LAS f32x4*)(lds + L_EX + (tid & 255) * 16) = (f32x4){-m, c0 * r, c0 * 0.044715f * r * r * r, sd * sd};
      LDS1(L_EX + 4096 + tid * 4) = __float_as_uint((__uint_as_float(cq[0][0]) + __uint_as_float(cq[1][0])) + (__uint_as_float(cq[2][0]) + __uint_as_float(cq[3][0]))); }
    BAR; WAIT_LB(B1); MMA(0, 1, B1); BAR;
    LDA(At, 1, 1);
    BAR; WAIT_L(0); MMA(1, 0, B0); MMA(1, 1, B1);
    asm volatile("s_waitcnt vmcnt(0)" : "+v"(f2)::"memory");
    if (wid == 0) { LDS1(L_EX + 6144) = __all(f2 == epoch); WAIT_L(0); }
    BAR;
    if (wr == 0) BAR;
    STAMP(2);
    f32x4 rc[8], cg[2], cu[2];
    UNR for (int i = 0; i < 8; i++) rc[i] = *(LAS f32x4*)(lds + L_EX + ((i >> 2) * 128 + wr * 64 + (i & 3) * 16 + fr) * 16);
    UNR for (int n = 0; n < 2; n++) { const int o = L_EX + 4096 + (wc * 32 + n * 16 + fq * 4) * 4;
      cg[n] = *(LAS f32x4*)(lds + o) + *(LAS f32x4*)(lds + o + 1024); cu[n] = *(LAS f32x4*)(lds + o + 512) + *(LAS f32x4*)(lds + o + 1536); }
    p3_done = __builtin_amdgcn_readfirstlane(LDS1(L_EX + 6144)) != 0;
    u32x4 zz[4];
    P3_LOAD;
    SCHED; WAIT_L(0);
    G2_VOFF;
    const int hoff = OFF_H + ((pm * 16 + wr * 4) * 112 + pn * 4 + wc) * 1024 + lane * 16;
    UNR for (int i = 0; i < 8; i++) { float hv[8];
      UNR for (int j = 0; j < 8; j++) { const float gg = __builtin_fmaf(rc[i][0], cg[j >> 2][j & 3], acc[i >> 2][0][i & 3][j >> 2][j & 3]);
        const float uu = __builtin_fmaf(rc[i][0], cu[j >> 2][j & 3], acc[i >> 2][1][i & 3][j >> 2][j & 3]);
        const float ex = __builtin_amdgcn_exp2f(gg * __builtin_fmaf(gg * gg, rc[i][2], rc[i][1]));
        hv[j] = gg * uu * __builtin_amdgcn_rcpf(__builtin_fmaf(ex, rc[i][3], rc[i][3])); }
      ST_SC1(((u32x4){pk_bf16(hv[0], hv[1]), pk_bf16(hv[2], hv[3]), pk_bf16(hv[4], hv[5]), pk_bf16(hv[6], hv[7])}), hoff + ((i >> 2) * 8 + (i & 3)) * 112 * 1024);
      SCHED; STD1(i, 0); STD1(i, 1); SCHED; }
    XSTAMP(3);
    asm volatile("s_waitcnt vmcnt(24)" : "+v"(zz[0]), "+v"(zz[1]), "+v"(zz[2]), "+v"(zz[3])::"memory");
    if (p3_done) { P3_RUN(24); XSTAMP(4); WAIT_V(2); XPUT(4); } else WAIT_V(0);
    BAR;
    if (tid == 0) FLAG_ST(OFF_FH + (pm * 32 + pn) * 128);
    XPUT(3);
  } else {
    LANES;
    WAIT_ALL; BAR;
    if (tid == 0) FLAG_ST(OFF_F1 + b * 128);
    UNR for (int z = 0; z < 2; z++) {
      const float mult = (float)(32 * (wid >> 2) + l31) * (1.0f / 64), b0 = (float)(16 * (wid & 3) + 4 * hh);
      float v[8];
      UNR for (int e = 0; e < 8; e++) v[e] = cosr(__builtin_fmaf(b0 + (float)((e & 3) + 8 * (e >> 2)), mult, -0.25f * z));
      LDS4((z * 8 + wid) * 1024 + lane * 16) = (u32x4){pk_bf16(v[0], v[1]), pk_bf16(v[2], v[3]), pk_bf16(v[4], v[5]), pk_bf16(v[6], v[7])}; }
    if (wid >= 4) { const float mult = (float)l31 * (1.0f / 32), b0 = (float)(8 * (wid & 3) + 4 * hh), ph = 0.25f;
      u32x4 w;
      UNR for (int e = 0; e < 4; e++) { const float a = __builtin_fmaf(b0 + (float)e, mult, ph); w[e] = pk_bf16(cosr(a), cosr(a - 0.25f)); }
      LDS4(L_FR + wid * 1024 + lane * 16) = w; }
    UNR for (int z = 0; z < 3; z++) {
      const float a0 = (float)(2 * (wid & 1) + 8 * (wid >> 1) + 4 * hh + (z == 1 ? 32 : 0)), m2 = (float)(l31 + (z == 2 ? 32 : 0)) * (1.0f / 2048);
      const float t0 = a0 * m2, t1 = t0 + m2;
      *(LAS u32x2*)(lds + L_TW + z * 4096 + (wid >> 1) * 1024 + lane * 16 + (wid & 1) * 8) = (u32x2){pk_f16(cosr(t0), cosr(t0 + 0.25f)), pk_f16(cosr(t1), cosr(t1 + 0.25f))}; }
    WAIT_L(0); BAR;
    u32x4 CC[8], SS[8];
    UNR for (int i = 0; i < 8; i++) { CC[i] = LDS4(i * 1024 + lane * 16); SS[i] = LDS4(8192 + i * 1024 + lane * 16); }
    if (wid < 4) POLLS(OFF_F1 + tid * 128, 4);
    WAIT_L(0); BAR;
    if (g == 0 && tid == 0) FLAG_ST(OFF_E);
    STAMP(2);
    u32x4 xc[8], xo[8];
    {
      u32x4 sx[16], sw[16];
      const int so = g * SLAB + wid * 16384 + lane * 16;
      UNR for (int i = 0; i < 16; i++) sx[i] = LD_SC1(OFF_X1 + so + i * 1024);
      UNR for (int i = 0; i < 16; i++) sw[i] = LD_SC1(OFF_WT + so + i * 1024);
      UNR for (int i = 0; i < 16; i++) LDS4(wid * 16384 + i * 1024 + lane * 16) = sx[i];
      if (g == 0) {
        u32x4 f1, f2;
        UNR for (int e = 0; e < 4; e++) { const int j = l31 + 32 * (8 * wid + 4 * hh + e);
          const unsigned long long p = __hip_atomic_load((GAS unsigned long long*)(ws + OFF_R + j * 16 + 8), __ATOMIC_RELAXED, __HIP_MEMORY_SCOPE_AGENT);
          const unsigned long long n = __hip_atomic_load((GAS unsigned long long*)(ws + OFF_R + ((2048 - j) & 2047) * 16 + 8), __ATOMIC_RELAXED, __HIP_MEMORY_SCOPE_AGENT);
          const float ar = bf_lo(p >> 32) + bf_lo(n >> 32), ai = bf_hi(p >> 32) - bf_hi(n >> 32), br = bf_lo(p) + bf_lo(n), bi = bf_hi(p) - bf_hi(n);
          f1[e] = pk_bf16(0.125f * (ar + br), 0.125f * (ai + bi)); f2[e] = pk_bf16(0.125f * (ar - br), 0.125f * (ai - bi)); }
        *(u32x4*)(ws + OFF_FF + tid * 16) = f1; *(u32x4*)(ws + OFF_FF + 8192 + tid * 16) = f2;
      }
      WAIT_L(0); BAR;
#define XBASES const int xb0_ = l31 * 64 + hh * 16384 + ((((wid >> 1) ^ ((l31 >> 1) & 3)) * 4 + ((2 * (wid & 1)) ^ (l31 >> 3))) * 4); \
      const int xb[4] = {opq(xb0_), opq(xb0_ + 65536), opq(xb0_ ^ 4), opq((xb0_ ^ 4) + 65536)}
      XBASES;
      UNR for (int i = 0; i < 32; i++) { const int o = (i >> 4) * 2048 + ((i >> 2) & 1) * 32768 + (i & 3) * 4096;
        xc[i >> 2][i & 3] = LDS1(xb[(i >> 3) & 1] + o); xo[i >> 2][i & 3] = LDS1(xb[2 + ((i >> 3) & 1)] + o); }
      WAIT_L(0); BAR;
      UNR for (int i = 0; i < 16; i++) LDS4(wid * 16384 + i * 1024 + lane * 16) = sw[i];
      WAIT_V(0); WAIT_L(0); BAR;
    }
    STAMP(3);
#pragma unroll 1
    for (int c = 0; c < 2; c++) {
      LANES;
      const int ex = opq(L_EX + lane * 16) - L_EX;
      u32x4 pr[4], pi[4], pn_[4];
      { u32x4 fri[8];
        UNR for (int i = 0; i < 8; i++) fri[i] = LDS4(ex + (L_FR + i * 1024));
        UNR for (int mu = 0; mu < 2; mu++) { f32x16 re = ZERO16, im = ZERO16;
          UNR for (int k = 0; k < 4; k++) { re = MF32(xc[mu * 4 + k], fri[k], re); im = MF32(xc[mu * 4 + k], fri[4 + k], im); }
          UNR for (int q = 0; q < 4; q++) { const u32x4 tw = LDS4(ex + (L_TW + mu * 4096 + q * 1024)); float r[4], i[4];
            UNR for (int e = 0; e < 4; e++) cmul(re[4 * q + e], im[4 * q + e], tw[e], r[e], i[e]);
            UNR for (int e = 0; e < 2; e++) { pr[mu * 2 + (q >> 1)][(q & 1) * 2 + e] = pk_bf16(r[2 * e], r[2 * e + 1]); pi[mu * 2 + (q >> 1)][(q & 1) * 2 + e] = pk_bf16(i[2 * e], i[2 * e + 1]); } } } }
      SCHED;
      UNR for (int k = 0; k < 4; k++) pn_[k] = pr[k] ^ 0x80008000u;
      u32x4 yr[4], yi[4], yn[4];
      if (g == 0 && wid == 0 && c == 0) {
        f32x16 re[2], im[2];
        UNR for (int nu = 0; nu < 2; nu++) { re[nu] = ZERO16; im[nu] = ZERO16;
          UNR for (int k = 0; k < 4; k++) { re[nu] = MF32(CC[nu * 4 + k], pr[k], re[nu]); im[nu] = MF32(CC[nu * 4 + k], pi[k], im[nu]);
            re[nu] = MF32(SS[nu * 4 + k], pi[k], re[nu]); im[nu] = MF32(SS[nu * 4 + k], pn_[k], im[nu]); } }
        SCHED;
        u32x4 ff[16];
        UNR for (int q = 0; q < 16; q++) ff[q] = *(const u32x4*)(ws + OFF_FF + ((q & 1) * 8 + (q >> 1)) * 1024 + lane * 16);
        SCHED;
        const int sb = opq(L_SC + l31 * 4 + hh * 512), mb = opq(L_SC + 8192 - 59 * 128 - l31 * 4 - hh * 512);
        UNR for (int i = 0; i < 32; i++) LDS1(sb + ((i & 3) + 8 * (i >> 2)) * 128) = pk_bf16(re[i >> 4][i & 15], im[i >> 4][i & 15]);
        UNR for (int q = 0; q < 8; q++) { const u32x4 f1 = ff[2 * q], f2 = ff[2 * q + 1];
          float r[4], i[4];
          UNR for (int e = 0; e < 4; e++) { const unsigned mm = LDS1(e + q == 0 ? (lane == 0 ? L_SC : mb + 59 * 128) : mb + (59 - e - 8 * q) * 128);
            const float xr = re[q >> 2][(q & 3) * 4 + e], xi = im[q >> 2][(q & 3) * 4 + e], mr = bf_lo(mm), mi = bf_hi(mm);
            r[e] = xr * bf_lo(f1[e]) - xi * bf_hi(f1[e]) + mr * bf_lo(f2[e]) + mi * bf_hi(f2[e]);
            i[e] = -xr * bf_hi(f1[e]) - xi * bf_lo(f1[e]) - mr * bf_hi(f2[e]) + mi * bf_lo(f2[e]); }
          UNR for (int e = 0; e < 2; e++) { yr[q >> 1][(q & 1) * 2 + e] = pk_bf16(r[2 * e], r[2 * e + 1]); yi[q >> 1][(q & 1) * 2 + e] = pk_bf16(i[2 * e], i[2 * e + 1]); } SCHED; }
      } else {
        const int wb0 = l31 * 64 + hh * 8192 + ((((2 * (wid & 1) + c) ^ ((l31 >> 1) & 3)) * 4 + ((wid >> 1) ^ (l31 >> 3))) * 4);
        const int wb[2] = {opq(wb0), opq(wb0 + 65536)};
        UNR for (int nu = 0; nu < 2; nu++) { f32x16 re = ZERO16, im = ZERO16;
          UNR for (int k = 0; k < 4; k++) { re = MF32(CC[nu * 4 + k], pr[k], re); im = MF32(CC[nu * 4 + k], pi[k], im);
            re = MF32(SS[nu * 4 + k], pi[k], re); im = MF32(SS[nu * 4 + k], pn_[k], im); }
          UNR for (int q = 0; q < 4; q++) { float r[4], i[4];
            UNR for (int e = 0; e < 4; e++) { const unsigned w = LDS1(wb[nu] + (e + 8 * q) * 2048); const float xr = re[4 * q + e], xi = im[4 * q + e];
              r[e] = __builtin_fmaf(xr, bf_lo(w), -(xi * bf_hi(w))); i[e] = __builtin_fmaf(-xr, bf_hi(w), -(xi * bf_lo(w))); }
            UNR for (int e = 0; e < 2; e++) { yr[nu * 2 + (q >> 1)][(q & 1) * 2 + e] = pk_bf16(r[2 * e], r[2 * e + 1]); yi[nu * 2 + (q >> 1)][(q & 1) * 2 + e] = pk_bf16(i[2 * e], i[2 * e + 1]); } } }
      }
      SCHED;
      UNR for (int k = 0; k < 4; k++) yn[k] = yr[k] ^ 0x80008000u;
      u32x4 up[8];
      UNR for (int mu = 0; mu < 2; mu++) { f32x16 re = ZERO16, im = ZERO16;
        UNR for (int k = 0; k < 4; k++) { re = MF32(yr[k], CC[mu * 4 + k], re); im = MF32(yi[k], CC[mu * 4 + k], im);
          re = MF32(yi[k], SS[mu * 4 + k], re); im = MF32(yn[k], SS[mu * 4 + k], im); }
        UNR for (int q = 0; q < 4; q++) { const u32x4 tw = LDS4(ex + (L_TW + mu * 8192 + q * 1024));
          UNR for (int e = 0; e < 4; e++) { float r, i; cmul(re[4 * q + e], im[4 * q + e], tw[e], r, i); up[mu * 4 + q][e] = pk_bf16(r, i); } } }
      SCHED;
      { u32x4 fri[8];
        UNR for (int i = 0; i < 8; i++) fri[i] = LDS4(ex + (L_FR + i * 1024));
        UNR for (int mu = 0; mu < 2; mu++) { f32x16 re = ZERO16, im = ZERO16;
          UNR for (int k = 0; k < 4; k++) { re = MF32(fri[k], up[mu * 4 + k], re); im = MF32(fri[4 + k], up[mu * 4 + k], im); }
          UNR for (int i = 0; i < 16; i++) { xc[mu * 4 + (i >> 2)][i & 3] = xo[mu * 4 + (i >> 2)][i & 3]; xo[mu * 4 + (i >> 2)][i & 3] = pk_bf16(re[i], im[i]); } } }
    }
    STAMP(4);
    BAR;
    {
    LANES;
    { XBASES;
      UNR for (int i = 0; i < 32; i++) { const int o = (i >> 4) * 2048 + ((i >> 2) & 1) * 32768 + (i & 3) * 4096;
        LDS1(xb[(i >> 3) & 1] + o) = xc[i >> 2][i & 3]; LDS1(xb[2 + ((i >> 3) & 1)] + o) = xo[i >> 2][i & 3]; } }
    WAIT_L(0); BAR;
    UNR for (int i = 0; i < 16; i++) ST_SC1(LDS4(wid * 16384 + i * 1024 + lane * 16), OFF_Z + g * SLAB + wid * 16384 + i * 1024 + lane * 16);
    TAB_T1;
    WAIT_V(0); WAIT_L(0); BAR;
    if (tid < 32) FLAG_ST(OFF_F2 + tid * 128 + g * 4);
    STAMP(5);
    G2_PRELOAD;
    SCHED;
    }
  }
  STAMP(7);
  if (!p3_done) {
    LANES;
    if (wid == 0) POLL1(OFF_F2 + rr * 128 + l31 * 4);
    BAR;
    u32x4 zz[4];
    P3_LOAD; P3_RUN(0);
  }
  STAMP(8);

  {
    LANES;
    const int kg = wid >> 2, rg = wid & 3;
    if (wid == 0) { const int np = nkt >> 1; POLL1(OFF_FH + ((tm >> 1) * 32 + (kt0 >> 1) + (lane < np ? lane : 0)) * 128); }
    G2_VOFF;
    const int va0 = OFF_H + ((tm * 8 + rg * 2 + kg) * 112 + kt0 * 2 + kg) * 1024 + lane * 16, va1 = OFF_H + ((tm * 8 + rg * 2 + (kg ^ 1)) * 112 + kt0 * 2 + kg) * 1024 + lane * 16;
    int bo[8];
    { int q = fr >> 2, p = lane & 3; int row = 32 * kg + 4 * fq + q; int base = row * 256 + (((p >> 1) ^ ((row & 7) << 1)) * 16) + 8 * (p & 1);
      UNR for (int n = 0; n < 8; n++) bo[n] = base ^ (n * 32); }
    const int row = tm * 128 + (rg * 2 + kg) * 16 + fr, colo = tn * 128 + (fq & 1) * 16 + (fq >> 1) * 8;
    const int po = OFF_P + tile * 32768 + wid * 4096 + lane * 16;
    const int eo = half ? OFF_T + row * (D_ * 2) + colo * 2 : po, es = half ? 64 : 1024;
    f32x4 acc[2][8];
    UNR for (int a = 0; a < 2; a++) UNR for (int n = 0; n < 8; n++) acc[a][n] = (f32x4){0, 0, 0, 0};
    u32x4 Ar[4][2], ev[4]; u32x2 B0[8][2], B1[8][2];
    if (!isG) BAR;
    UNR for (int n = 0; n < 8; n++) { TRR(B0[n][0], bo[n], 0); TRR(B0[n][1], bo[n], 4096); }
    BAR;
    STAMP(9);
#define LDH1(sl, t, i) asm volatile("buffer_load_dwordx4 %0, %1, %2, %3 offen sc1" : "=v"(Ar[sl][i]) : "v"((i) ? va1 : va0), "s"(rsW), "s"((t) * 2048) : "memory")
#define LDE(u) asm volatile("buffer_load_dwordx4 %0, %1, %2, %3 offen sc1" : "=v"(ev[u]) : "v"(eo), "s"(rsW), "s"((u) * es) : "memory")
#define WAIT_LD(B) asm volatile("s_waitcnt lgkmcnt(0)" : "+v"(B[0][0]), "+v"(B[0][1]), "+v"(B[1][0]), "+v"(B[1][1]), "+v"(B[2][0]), "+v"(B[2][1]), "+v"(B[3][0]), "+v"(B[3][1]), \
   "+v"(B[4][0]), "+v"(B[4][1]), "+v"(B[5][0]), "+v"(B[5][1]), "+v"(B[6][0]), "+v"(B[6][1]), "+v"(B[7][0]), "+v"(B[7][1])::"memory")
#define WAIT_VA(n, sl) asm volatile("s_waitcnt vmcnt(" #n ")" : "+v"(Ar[sl][0]), "+v"(Ar[sl][1])::"memory")
#define FLIP do { UNR for (int n = 0; n < 8; n++) bo[n] ^= 65536; } while (0)
#define ITER(t, Bc, Bn, sl, so, VC, ISB, ISA, RD, DOBAR) do { WAIT_VA(VC, sl); WAIT_LD(Bc); if (DOBAR) BAR; SCHED; \
    UNR for (int n = 0; n < 8; n++) { \
      if (RD) { TRR(Bn[n][0], bo[n], so); TRR(Bn[n][1], bo[n], so + 4096); } \
      acc[n & 1][n] = MF16(CAT(Bc[n]), Ar[sl][n & 1], acc[n & 1][n]); SCHED; \
      acc[(n & 1) ^ 1][n] = MF16(CAT(Bc[n]), Ar[sl][(n & 1) ^ 1], acc[(n & 1) ^ 1][n]); SCHED; \
      if (ISB && n == 0) STD1((t) + 7, 0); if (ISB && n == 2) STD1((t) + 7, 1); \
      if (ISA == 1 && n == 4) LDH1(((sl) + 3) & 3, (t) + 3, 0); if (ISA == 1 && n == 6) LDH1(((sl) + 3) & 3, (t) + 3, 1); \
      if (ISA == 2 && n < 4) LDE(n); SCHED; } } while (0)
    LDH1(0, 0, 0); LDH1(0, 0, 1); LDH1(1, 1, 0); LDH1(1, 1, 1); LDH1(2, 2, 0); LDH1(2, 2, 1);
    ITER(0, B0, B1, 0, 16384, 4, 0, 1, 1, 1);
    if (tid == 0) FLAG_ST(OFF_F3 + b * 128);
    ITER(1, B1, B0, 1, 32768, 4, 1, 1, 1, 0);
    ITER(2, B0, B1, 2, 49152, 6, 1, 1, 1, 1); FLIP; ITER(3, B1, B0, 3, 0, 8, 1, 1, 1, 0);
    int t;
    for (t = 4; t < nkt - 8; t += 4) {
      ITER(t, B0, B1, 0, 16384, 8, 1, 1, 1, 1); ITER(t + 1, B1, B0, 1, 32768, 8, 1, 1, 1, 0);
      ITER(t + 2, B0, B1, 2, 49152, 8, 1, 1, 1, 1); FLIP; ITER(t + 3, B1, B0, 3, 0, 8, 1, 1, 1, 0); }
    ITER(t, B0, B1, 0, 16384, 8, 1, 1, 1, 1); ITER(t + 1, B1, B0, 1, 32768, 8, 0, 1, 1, 0);
    ITER(t + 2, B0, B1, 2, 49152, 6, 0, 1, 1, 1); FLIP; ITER(t + 3, B1, B0, 3, 0, 4, 0, 1, 1, 0);
    ITER(t + 4, B0, B1, 0, 16384, 4, 0, 1, 1, 1); ITER(t + 5, B1, B0, 1, 32768, 4, 0, 0, 1, 0);
    if (wid == 0) POLL1(half ? OFF_F3 + (16 * tm + fr) * 128 : OFF_FP + tile * 128);
    ITER(t + 6, B0, B1, 2, 49152, 2, 0, 2, 1, 1); ITER(t + 7, B1, B0, 3, 0, 4, 0, 0, 0, 1);
    STAMP(10);
    UNR for (int n = 0; n < 8; n++) *(LAS f32x4*)(lds + wid * 8192 + n * 1024 + lane * 16) = acc[1][n];
    WAIT_L(0); BAR;
    UNR for (int n = 0; n < 8; n++) acc[0][n] += *(LAS f32x4*)(lds + (wid ^ 4) * 8192 + n * 1024 + lane * 16);
    UNR for (int u = 0; u < 4; u++) UNR for (int j = 0; j < 4; j++) {
      auto sw = __builtin_amdgcn_permlane16_swap(__float_as_uint(acc[0][2 * u][j]), __float_as_uint(acc[0][2 * u + 1][j]), false, false);
      acc[0][2 * u][j] = __uint_as_float(sw[0]); acc[0][2 * u + 1][j] = __uint_as_float(sw[1]); }
    asm volatile("s_waitcnt vmcnt(0)" : "+v"(ev[0]), "+v"(ev[1]), "+v"(ev[2]), "+v"(ev[3])::"memory");
    UNR for (int u = 0; u < 4; u++) { const f32x4 lo = acc[0][2 * u], hi = acc[0][2 * u + 1];
      const u32x4 w = {pk_bf16(lo[0] + bf_lo(ev[u][0]), lo[1] + bf_hi(ev[u][0])), pk_bf16(lo[2] + bf_lo(ev[u][1]), lo[3] + bf_hi(ev[u][1])),
                       pk_bf16(hi[0] + bf_lo(ev[u][2]), hi[1] + bf_hi(ev[u][2])), pk_bf16(hi[2] + bf_lo(ev[u][3]), hi[3] + bf_hi(ev[u][3]))};
      if (half) ST_SC1(w, po + u * 1024); else *(u32x4*)(O + row * (D_ * 2) + (colo + 32 * u) * 2) = w; }
    if (half) { WAIT_V(0); BAR; if (tid == 0) FLAG_ST(OFF_FP + tile * 128); }
    STAMP(11);
  }
}

extern "C" void kernel_launch(void* const* d_in, const int* in_sizes, int n_in, void* d_out, int out_size, void* d_ws, size_t ws_size, hipStream_t stream) {
  (void)in_sizes; (void)n_in; (void)out_size; (void)ws_size;
  (void)hipFuncSetAttribute((const void*)fused_k, hipFuncAttributeMaxDynamicSharedMemorySize, LDS_BYTES);
  fused_k<<<256, 512, LDS_BYTES, stream>>>((const char*)d_in[0], (const char*)d_in[1], (const char*)d_in[2], (const char*)d_in[4],
                                           (const char*)d_in[5], (const char*)d_in[6], (char*)d_out, (char*)d_ws);
}
```

```cpp
#include <hip/hip_runtime.h>
#include <stdint.h>
#define E1 6
#define E4

#define LAS __attribute__((address_space(3)))
#define GAS __attribute__((address_space(1)))
typedef short bf16x8 __attribute__((ext_vector_type(8)));
typedef float f32x4 __attribute__((ext_vector_type(4)));
typedef float f32x16 __attribute__((ext_vector_type(16)));
typedef unsigned u32x4 __attribute__((ext_vector_type(4)));
typedef unsigned u32x2 __attribute__((ext_vector_type(2)));
typedef u32x2 u32x2u __attribute__((aligned(2)));
typedef __bf16 bf16x2v __attribute__((ext_vector_type(2)));
typedef _Float16 h16x2 __attribute__((ext_vector_type(2)));
typedef float f32x2 __attribute__((ext_vector_type(2)));

constexpr int S_ = 2048, D_ = 1024, F_ = 3584;
constexpr int OFF_E = 0, OFF_F2 = 4096, OFF_FP = 16384, OFF_R = 32768, OFF_FF = 65536, OFF_CS = 131072, OFF_F1 = 655360, OFF_F3 = OFF_F1 + 32768, OFF_FH = OFF_F3 + 32768;
constexpr int SLAB = 135424;
constexpr int OFF_X1 = 1 << 20, OFF_WT = 6 << 20, OFF_Z = 11 << 20, OFF_T = 16 << 20, OFF_P = 20 << 20, OFF_H = 24 << 20, WS_USED = 40 << 20;
constexpr int L_EX = 131072, L_T1 = L_EX + 8192, L_G1 = L_EX + 12288, L_FR = L_EX + 16384, L_SC = L_EX + 24576, LDS_BYTES = 163840;
constexpr int L_O1 = L_EX, L_O2 = L_SC, L_TC = L_EX + 20480;
constexpr int NA = 32;
constexpr unsigned SPIN_CAP = 1u << 18;

__device__ __forceinline__ unsigned pk_bf16(float lo, float hi) { f32x2 v = {lo, hi}; return __builtin_bit_cast(unsigned, __builtin_convertvector(v, bf16x2v)); }
__device__ __forceinline__ unsigned pk_f16(float lo, float hi) { h16x2 v = {(_Float16)lo, (_Float16)hi}; return __builtin_bit_cast(unsigned, v); }
__device__ __forceinline__ float bf_lo(unsigned u) { return __uint_as_float(u << 16); }
__device__ __forceinline__ float bf_hi(unsigned u) { return __uint_as_float(u & 0xffff0000u); }
__device__ __forceinline__ float rbf(float f) { return bf_lo(pk_bf16(f, f)); }
__device__ __forceinline__ float cosr(float rev) { return __builtin_amdgcn_cosf(rev); }
__device__ __forceinline__ float dot2(unsigned a, unsigned b, float c) { return __builtin_amdgcn_fdot2_f32_bf16(__builtin_bit_cast(bf16x2v, a), __builtin_bit_cast(bf16x2v, b), c, false); }
__device__ __forceinline__ void cmul(float re, float im, unsigned t, float& r2, float& i2) {
  h16x2 tw = __builtin_bit_cast(h16x2, t);
  float a = __builtin_fmaf(im, (float)tw[1], 0.0f), b = __builtin_fmaf(im, (float)tw[0], 0.0f);
  r2 = __builtin_fmaf(re, (float)tw[0], -a); i2 = __builtin_fmaf(re, (float)tw[1], b);
  asm("" : "+v"(r2)); asm("" : "+v"(i2));
}
__device__ __forceinline__ float TWH(unsigned t, int h) { h16x2 tw = __builtin_bit_cast(h16x2, t); return (float)tw[h]; }
template <int CTRL> __device__ __forceinline__ float dppf(float v) { return __builtin_bit_cast(float, __builtin_amdgcn_update_dpp(0, __builtin_bit_cast(int, v), CTRL, 0xf, 0xf, true)); }
__device__ __forceinline__ float par_sum(float v) {
  v += dppf<0x4E>(v); v += dppf<0x124>(v); v += dppf<0x128>(v);
  auto a = __builtin_amdgcn_permlane16_swap(__float_as_uint(v), __float_as_uint(v), false, false); v = __uint_as_float(a[0]) + __uint_as_float(a[1]);
  auto b = __builtin_amdgcn_permlane32_swap(__float_as_uint(v), __float_as_uint(v), false, false); return __uint_as_float(b[0]) + __uint_as_float(b[1]);
}
__device__ __forceinline__ float rdl(float v, int l) { return __builtin_bit_cast(float, __builtin_amdgcn_readlane(__builtin_bit_cast(int, v), l)); }

__device__ __forceinline__ int opq(int v) { asm volatile("" : "+v"(v)); return v; }
__device__ __forceinline__ int lane_id() { int v; asm volatile("v_mbcnt_lo_u32_b32 %0, -1, 0\n\tv_mbcnt_hi_u32_b32 %0, -1, %0" : "=v"(v)); __builtin_assume(v >= 0 && v < 64); return v; }
#define LANES const int lane = lane_id(), tid = wid * 64 + lane, l31 = lane & 31, hh = lane >> 5, fr = lane & 15, fq = lane >> 4; (void)tid; (void)l31; (void)hh; (void)fr; (void)fq
#define BC8(x) __builtin_bit_cast(bf16x8, x)
#define MF32(a, b, c) __builtin_amdgcn_mfma_f32_32x32x16_bf16(BC8(a), BC8(b), c, 0, 0, 0)
#define MF16(a, b, c) __builtin_amdgcn_mfma_f32_16x16x32_bf16(BC8(a), BC8(b), c, 0, 0, 0)
#define WAIT_V(n) asm volatile("s_waitcnt vmcnt(" #n ")" ::: "memory")
#define WAIT_L(n) asm volatile("s_waitcnt lgkmcnt(" #n ")" ::: "memory")
#define WAIT_ALL do { __builtin_amdgcn_s_waitcnt(0x0070); asm volatile("" ::: "memory"); } while (0)
#define BAR __builtin_amdgcn_s_barrier()
#define SCHED __builtin_amdgcn_sched_barrier(0)
#define UNR _Pragma("unroll")
#define LDS4(off) (*(LAS u32x4*)(lds + (off)))
#define LDS1(off) (*(LAS unsigned*)(lds + (off)))
#define ST_SC1(v, off) __builtin_amdgcn_raw_buffer_store_b128(v, rsW, off, 0, 16)
#define LD_SC1(off) __builtin_amdgcn_raw_buffer_load_b128(rsW, off, 0, 16)
#define FLAG_ST(off) __hip_atomic_store((GAS unsigned*)(ws + (off)), epoch, __ATOMIC_RELAXED, __HIP_MEMORY_SCOPE_AGENT)
#define FLAG_LD(off) __hip_atomic_load((GAS unsigned*)(ws + (off)), __ATOMIC_RELAXED, __HIP_MEMORY_SCOPE_AGENT)
#define POLLS(off, slp) do { const int po_ = (off); _Pragma("unroll 1") for (unsigned sp_ = 0; sp_ < SPIN_CAP && FLAG_LD(po_) != epoch; ++sp_) __builtin_amdgcn_s_sleep(slp); \
    __builtin_amdgcn_fence(__ATOMIC_ACQUIRE, "wavefront"); } while (0)
#define POLL1(off) POLLS(off, 1)
#define TRR(dst, base, off) asm volatile("ds_read_b64_tr_b16 %0, %1 offset:%2" : "=v"(dst) : "v"(base), "i"(off) : "memory")
#define CAT(x) __builtin_shufflevector(x[0], x[1], 0, 1, 2, 3)
#define STAMP(i)
#define XSTAMP(i)
#define XPUT(i)
static const f32x16 ZERO16 = {0, 0, 0, 0, 0, 0, 0, 0, 0, 0, 0, 0, 0, 0, 0, 0};

__global__ __launch_bounds__(512, 2) void fused_k(const char* __restrict__ X, const char* __restrict__ Wr, const char* __restrict__ Wi,
                                                  const char* __restrict__ Wg, const char* __restrict__ Wu, const char* __restrict__ Wd,
                                                  char* __restrict__ O, char* ws) {
  extern __shared__ __attribute__((aligned(16))) char smem_[];
  LAS char* lds = (LAS char*)smem_;
  const int b = blockIdx.x, xg = b & 7, rr = b >> 3;
  const bool isG = rr < 28;
  const int g = xg * 4 + (rr & 3);
  const int wid = __builtin_amdgcn_readfirstlane(threadIdx.x >> 6);
  const auto rsW = __builtin_amdgcn_make_buffer_rsrc(ws, 0, WS_USED, 0x00020000);
  STAMP(0);

  const int pm = (xg & 1) * 4 + (rr & 3), pn = (xg >> 1) * 7 + (rr >> 2);
  const int wr = wid >> 2, wc = wid & 3, kr = isG ? (xg & 1) * 8 : 0;
  const int g28 = g < 28 ? g : g - 28;
  const char* cA = isG ? X + pm * 256 * (D_ * 2) : Wd + (g >> 3) * 256 * (D_ * 2) + (g & 7) * 256;
  const char* cG = isG ? Wg + pn * 256 : Wd + 384 * (F_ * 2) + g28 * 256;
  const char* cU = isG ? Wu + pn * 256 : Wd + 512 * (F_ * 2) + g28 * 256;
  const unsigned ldsw = wid * 1024;
#define G1_VOFF unsigned voffA, voffB; { int row = wid * 8 + (lane >> 3); voffA = row * (D_ * 2) + (((lane & 7) ^ (lane >> 3)) * 16); } \
  { int row = wid * 4 + fq; int swz = ((row & 3) | ((row >> 1) & 4)) << 1; voffB = row * (F_ * 2) + ((fr ^ swz) * 16); }
#define SA(bf, h) (((bf) * 2 + (h)) * 16384)
#define SB(bf, h) ((4 + (bf) * 2 + (h)) * 16384)
#define STA(bufoff, h, t) do { const char* g_ = cA + (h) * 128 * (D_ * 2) + ((t) ^ kr) * 128; UNR for (int i_ = 0; i_ < 2; ++i_) \
    __builtin_amdgcn_global_load_lds((const unsigned*)(g_ + i_ * 64 * (D_ * 2) + voffA), (LAS unsigned*)(lds + (bufoff) + ldsw + i_ * 8192), 16, 0, 0); } while (0)
#define STB(bufoff, base, t) do { const char* g_ = (base) + ((t) ^ kr) * (64 * F_ * 2); UNR for (int i_ = 0; i_ < 2; ++i_) \
    __builtin_amdgcn_global_load_lds((const unsigned*)(g_ + i_ * 32 * (F_ * 2) + voffB), (LAS unsigned*)(lds + (bufoff) + ldsw + i_ * 8192), 16, 0, 0); } while (0)
  const int s = b * 8 + wid, sq = (wid >> 1) & 3, se = b & 3;
  const int tok = (s & ~63) | ((s & 31) << 1) | ((s >> 5) & 1);
#define WNT ""
#define TOUCH(p) __builtin_amdgcn_global_load_lds((const unsigned*)(p), (LAS unsigned*)(lds + L_SC + wid * 256), 4, 0, 0)
#define GLD(dst, ins, voff, sbase, mod) asm volatile(ins " %0, %1, %2 " mod : "=v"(dst) : "v"(voff), "s"(sbase) : "memory")
#define TAB_TW(dst, ca, cb, ml) do { const float m2 = (ml), t0 = (float)((ca) * (2 * (wid & 1) + 8 * (wid >> 1) + 4 * hh) + (cb)) * m2, t1 = t0 + (float)(ca) * m2; \
    *(LAS u32x2*)(lds + (dst) + (wid >> 1) * 1024 + lane * 16 + (wid & 1) * 8) = (u32x2){pk_f16(cosr(t0), cosr(t0 + 0.25f)), pk_f16(cosr(t1), cosr(t1 + 0.25f))}; } while (0)
#define TAB_G1 do { const int a = (l31 & 19) | ((l31 & 4) << 1) | ((l31 & 8) >> 1), sx_ = wid < 4 ? se : 0; \
    const float mult = (float)(wid < 4 ? a : l31) * (1.0f / 32), b0 = (float)(8 * (wid < 4 ? wid & 1 : wid & 3) + 4 * hh), ph = wid < 4 ? 0.25f * (float)(wid >> 1) : 0.f; \
    u32x4 w; \
    UNR for (int e = 0; e < 4; e++) { const float t = __builtin_fmaf(b0 + (float)(e ^ sx_), mult, ph); w[e] = pk_bf16(cosr(t), cosr(t - 0.25f)); } \
    LDS4(L_G1 + wid * 1024 + lane * 16) = w; } while (0)
  unsigned epoch;
  {
  LANES;
  unsigned e0, w5r, w5i; u32x4 xa[2]; u32x2 wrv[2], wiv[2];
  { const int xo_ = l31 * 64 + hh * 16;
    GLD(xa[0], "global_load_dwordx4", xo_, X + tok * 2048, ""); GLD(xa[1], "global_load_dwordx4", xo_, X + tok * 2048, "offset:32");
    GLD(e0, "global_load_dword", 0, ws + OFF_E, "sc1"); }
  TOUCH(ws + (wid * 5 + (lane < 5 ? lane : 0)) * (1 << 20) + 4096);
#define E4N 16
  G1_VOFF;
  SCHED;
  STB(SB(0, 0), cG, 0); STA(SA(0, 0), 0, 0); STB(SB(0, 1), cU, 0); STA(SA(0, 1), 1, 0);
  SCHED;
  {
    const int e2 = ((l31 >> 1) & 1) + 2 * ((l31 >> 3) & 1), k2 = 4 * (2 * (l31 >> 4) + ((l31 >> 2) & 1)) + (e2 ^ se);
    const float mult = (float)(wid < 4 ? l31 : k2) * (1.0f / 32);
    const float ph = 0.25f * (float)(wid < 4 ? (wid >> 1) : (l31 & 1) - ((wid >> 1) & 1));
    const float b0 = (float)(16 * (wid & 1) + 4 * hh);
    float v[8];
    UNR for (int e = 0; e < 8; e++) v[e] = cosr(__builtin_fmaf(b0 + (float)((e & 3) + 8 * (e >> 2)), mult, ph));
    LDS4(L_EX + wid * 1024 + lane * 16) = (u32x4){pk_bf16(v[0], v[1]), pk_bf16(v[2], v[3]), pk_bf16(v[4], v[5]), pk_bf16(v[6], v[7])};
    TAB_TW(L_T1, 1, 0, (float)l31 * (1.0f / 1024));
  }
  WAIT_L(0); BAR;
  XSTAMP(12);
  { const int wo_ = ((lane >> 2) & 7) * 8 + (lane & 3) * 64 + hh * 256;
    GLD(wrv[0], "global_load_dwordx2", wo_, Wr + s * 1026, WNT); GLD(wiv[0], "global_load_dwordx2", wo_, Wi + s * 1026, WNT);
    GLD(wrv[1], "global_load_dwordx2", wo_, Wr + s * 1026, "offset:512 " WNT); GLD(wiv[1], "global_load_dwordx2", wo_, Wi + s * 1026, "offset:512 " WNT);
    GLD(w5r, "global_load_ushort", 0, Wr + s * 1026, "offset:1024"); GLD(w5i, "global_load_ushort", 0, Wi + s * 1026, "offset:1024"); }
  {
    u32x4 tb[12];
    UNR for (int i = 0; i < 12; i++) tb[i] = LDS4(L_EX + i * 1024 + lane * 16);
    TAB_G1;
    u32x4 idm[2];
    UNR for (int j = 0; j < 2; j++) { const int d = l31 - 16 * j - 8 * hh;
      UNR for (int p = 0; p < 4; p++) idm[j][p] = (d >> 1) == p ? (0x3f80u << (16 * (d & 1))) : 0u; }
    asm volatile("s_waitcnt vmcnt(%2)" : "+v"(xa[0]), "+v"(xa[1]) : "n"(E4N) : "memory");
    XSTAMP(13);
    f32x16 xt = MF32(xa[0], idm[0], ZERO16); xt = MF32(xa[1], idm[1], xt);
    float s0 = 0.f, s1 = 0.f;
    UNR for (int j = 0; j < 2; j++) UNR for (int p = 0; p < 4; p++) { s0 = dot2(xa[j][p], 0x3f803f80u, s0); s1 = dot2(xa[j][p], 0xbf803f80u, s1); }
    const float sv = par_sum(((lane & 1) ? s1 : s0) + dppf<0xB1>((lane & 1) ? s0 : s1));
    const float mu = rdl(sv, 0) * (1.0f / D_), alt = rdl(sv, 1), m = rbf(mu);
    float q = 0.f;
    UNR for (int i = 0; i < 16; i++) { xt[i] -= m; q = __builtin_fmaf(xt[i], xt[i], q); }
    u32x4 df[2];
    UNR for (int i = 0; i < 8; i++) df[i >> 2][i & 3] = pk_bf16(xt[2 * i], xt[2 * i + 1]);
    f32x16 tr = MF32(df[0], tb[0], ZERO16), ti = MF32(df[0], tb[2], ZERO16);
    tr = MF32(df[1], tb[1], tr); ti = MF32(df[1], tb[3], ti);
    q = par_sum(q + dppf<0xB1>(q));
    const float var = fmaxf(q * (1.0f / D_) - (mu - m) * (mu - m), 0.f);
    const float sd = rbf(__builtin_amdgcn_sqrtf(rbf(rbf(var) + 1e-5f))), rs = __builtin_amdgcn_rcpf(sd);
    SCHED; STB(SB(1, 0), cG, 1); STA(SA(1, 0), 0, 1); STB(SB(1, 1), cU, 1); SCHED;
    u32x4 br[2], bi[2];
    UNR for (int i = 0; i < 8; i++) { float r0, i0, r1, i1;
      cmul(tr[2 * i], ti[2 * i], tb[8 + (i >> 1)][(2 * i) & 3], r0, i0); cmul(tr[2 * i + 1], ti[2 * i + 1], tb[8 + (i >> 1)][(2 * i + 1) & 3], r1, i1);
      br[i >> 2][i & 3] = pk_bf16(r0, r1); bi[i >> 2][i & 3] = pk_bf16(i0, i1); }
    f32x16 xo = MF32(tb[4], br[0], ZERO16); xo = MF32(tb[6], bi[0], xo); xo = MF32(tb[5], br[1], xo); xo = MF32(tb[7], bi[1], xo);
    UNR for (int e = 0; e < 4; e++) xo[2 * e + 1] = (lane == 0 && se == e) ? alt : xo[2 * e + 1];
    XSTAMP(15);
    asm volatile("s_waitcnt vmcnt(6)" : "+v"(e0), "+v"(wrv[0]), "+v"(wrv[1]), "+v"(wiv[0]), "+v"(wiv[1]), "+v"(w5r), "+v"(w5i)::"memory");
    XSTAMP(14);
    epoch = __builtin_amdgcn_readfirstlane(e0) * 0x9E3779B1u + 0x7F4A7C15u;
    const int xoff = OFF_X1 + ((l31 >> 2) * 4 + hh) * SLAB + s * 64 + (((lane & 3) ^ sq) * 16);
    UNR for (int u = 0; u < 2; u++) { u32x4 w;
      UNR for (int e = 0; e < 4; e++) w[e] = pk_bf16(xo[8 * u + 2 * e] * rs, xo[8 * u + 2 * e + 1] * rs);
      ST_SC1(w, xoff + 2 * u * SLAB); }
    UNR for (int u = 0; u < 2; u++) { u32x4 w;
      UNR for (int e = 0; e < 4; e++) w[e] = __builtin_amdgcn_perm(wiv[u][e >> 1], wrv[u][e >> 1], (e & 1) ? 0x07060302u : 0x05040100u);
      ST_SC1(w, xoff + (OFF_WT - OFF_X1) + 2 * u * SLAB); }
    u32x4 rec = {pk_bf16(m, sd), epoch, w5r | (w5i << 16), __builtin_amdgcn_perm(wiv[0][0], wrv[0][0], 0x05040100u)};
    ST_SC1(rec, lane == 0 ? OFF_R + s * 16 : WS_USED);
    XPUT(12); XPUT(13); XPUT(14); XPUT(15);
  }
  }
  SCHED;
  STAMP(1);

  const int half = xg & 1, tm = (xg >> 1) * 4 + (rr & 3), tn = rr >> 2, tile = tm * 8 + tn;
  const int kt0 = half ? NA : 0, nkt = half ? 56 - NA : NA;
  const char* cB = Wd + (size_t)kt0 * 64 * (D_ * 2) + tn * 256;
#define G2_VOFF unsigned voffD; { int row = wid * 4 + fq; voffD = row * (D_ * 2) + ((fr ^ ((row & 7) << 1)) * 16); }
#define STD1(t, i) __builtin_amdgcn_global_load_lds((const unsigned*)(cB + (size_t)(t) * 64 * (D_ * 2) + (i) * 32 * (D_ * 2) + voffD), (LAS unsigned*)(lds + (((t) & 7) * 16384) + ldsw + (i) * 8192), 16, 0, 0)
#define G2_PRELOAD do { G2_VOFF; UNR for (int t_ = 0; t_ < 8; t_++) { STD1(t_, 0); STD1(t_, 1); } } while (0)

#define BLD(dst, ins, voff, mod) asm volatile(ins " %0, %1, %2, 0 offen " mod : "=v"(dst) : "v"(voff), "s"(rsW) : "memory")
#define P3_LOAD do { const int zo_ = OFF_Z + ((l31 >> 2) * 4 + hh) * SLAB + s * 64 + (((lane & 3) ^ sq) * 16), xo_ = l31 * 64 + hh * 16; \
    BLD(zz[0], "buffer_load_dwordx4", zo_, "sc1"); BLD(zz[1], "buffer_load_dwordx4", zo_ + 2 * SLAB, "sc1"); \
    GLD(zz[2], "global_load_dwordx4", xo_, X + tok * 2048, ""); GLD(zz[3], "global_load_dwordx4", xo_, X + tok * 2048, "offset:32"); } while (0)
#define P3_RUN(VC) do { u32x4 g1[4], tw3[4], frr[4]; \
    { const int ex = opq(L_EX + lane * 16) - L_EX, ea = opq(L_EX + ((lane & 51) | ((lane & 4) << 1) | ((lane & 8) >> 1)) * 16) - L_EX; \
      UNR for (int i = 0; i < 4; i++) { g1[i] = LDS4(ex + (L_G1 + i * 1024)); tw3[i] = LDS4(ea + (L_T1 + i * 1024)); frr[i] = LDS4(ex + (L_FR + i * 1024)); } } \
    const float sgn = hh ? 0.f : ((lane & 1) ? 1.f : -1.f); \
    asm volatile("s_waitcnt vmcnt(" #VC ")" : "+v"(zz[0]), "+v"(zz[1]), "+v"(zz[2]), "+v"(zz[3])::"memory"); \
    f32x16 re = MF32(zz[0], g1[0], ZERO16), im = MF32(zz[0], g1[2], ZERO16); \
    re = MF32(zz[1], g1[1], re); im = MF32(zz[1], g1[3], im); \
    const unsigned zq = __builtin_amdgcn_readlane((se & 2) ? ((se & 1) ? zz[0][3] : zz[0][2]) : ((se & 1) ? zz[0][1] : zz[0][0]), 0); \
    re[0] = __builtin_fmaf(sgn, bf_hi(zq), re[0]); \
    u32x4 up[4]; \
    UNR for (int i = 0; i < 16; i++) { float a_, c_; cmul(re[i], im[i], tw3[i >> 2][i & 3], a_, c_); up[i >> 2][i & 3] = pk_bf16(a_, c_); } \
    f32x16 y = ZERO16; \
    UNR for (int k = 0; k < 4; k++) y = MF32(up[k], frr[k], y); \
    u32x4 o[2]; \
    UNR for (int i = 0; i < 8; i++) { const unsigned xv = zz[2 + (i >> 2)][i & 3]; \
      o[i >> 2][i & 3] = pk_bf16(__builtin_fmaf(y[2 * i], 0x1p-20f, bf_lo(xv)), __builtin_fmaf(y[2 * i + 1], 0x1p-20f, bf_hi(xv))); } \
    UNR for (int e = 0; e < 4; e++) { auto sw = __builtin_amdgcn_permlane16_swap(o[0][e], o[1][e], false, false); o[0][e] = sw[0]; o[1][e] = sw[1]; } \
    const int to = OFF_T + tok * 2048 + fr * 64 + hh * 16 + (fq & 1) * 32; \
    ST_SC1(o[0], to); ST_SC1(o[1], to + 1024); } while (0)
  bool p3_done = false;

  if (isG) {
    LANES; G1_VOFF;
    const int aoff0 = (wr * 64 + fr) * 128 + ((fq ^ (fr & 7)) * 16), aoff1 = aoff0 ^ 64;
    int bo0, bo1;
    { int q = fr >> 2, p = lane & 3; int row = 8 * fq + q; int swz = ((row & 3) | ((row >> 1) & 4)) << 1;
      bo0 = row * 256 + (((4 * wc + (p >> 1)) ^ swz) * 16) + 8 * (p & 1) + 65536; bo1 = bo0 ^ 32; }
#define LDA(dst, bf, h) do { UNR for (int m = 0; m < 4; ++m) { dst[m][0] = LDS4(SA(bf, h) + aoff0 + m * 2048); dst[m][1] = LDS4(SA(bf, h) + aoff1 + m * 2048); } } while (0)
#define LDB(dst, bf, h) do { TRR(dst[0][0][0], bo0, SB(bf, h) - 65536); TRR(dst[0][0][1], bo0, SB(bf, h) - 65536 + 1024); \
    TRR(dst[1][0][0], bo1, SB(bf, h) - 65536); TRR(dst[1][0][1], bo1, SB(bf, h) - 65536 + 1024); \
    TRR(dst[0][1][0], bo0, SB(bf, h) - 65536 + 8192); TRR(dst[0][1][1], bo0, SB(bf, h) - 65536 + 9216); \
    TRR(dst[1][1][0], bo1, SB(bf, h) - 65536 + 8192); TRR(dst[1][1][1], bo1, SB(bf, h) - 65536 + 9216); } while (0)
#define WAIT_LB(B) asm volatile("s_waitcnt lgkmcnt(0)" : "+v"(B[0][0][0]), "+v"(B[0][0][1]), "+v"(B[0][1][0]), "+v"(B[0][1][1]), "+v"(B[1][0][0]), "+v"(B[1][0][1]), "+v"(B[1][1][0]), "+v"(B[1][1][1])::"memory")
#define MMA(ai, bj, Bt) do { __builtin_amdgcn_s_setprio(1); UNR for (int i = 0; i < 16; ++i) { const int m = i >> 2, n = ((i >> 1) ^ (i >> 2)) & 1, k = (i ^ (i >> 1)) & 1; \
    acc[ai][bj][m][n] = MF16(CAT(Bt[n][k]), At[m][k], acc[ai][bj][m][n]); SCHED; } __builtin_amdgcn_s_setprio(0); } while (0)
#define CSUM(on, Bt) do { if (on) { UNR for (int i = 0; i < 16; i++) cs[i & 1] = dot2(Bt[i & 1][(i >> 1) & 1][(i >> 2) & 1][i >> 3], 0x3f803f80u, cs[i & 1]); } SCHED; } while (0)
    f32x4 acc[2][2][4][2];
    UNR for (int a = 0; a < 2; a++) UNR for (int c = 0; c < 2; c++) UNR for (int m = 0; m < 4; m++) UNR for (int n = 0; n < 2; n++) acc[a][c][m][n] = (f32x4){0, 0, 0, 0};
    float cs[2] = {0.f, 0.f};
    const int tc = (rr & 3) * 2, tc0 = wr ? -1 : tc, tc1 = wr ? tc : -1;
    u32x4 At[4][2]; u32x2 B0[2][2][2], B1[2][2][2];
    BAR;
    if (wr == 1) BAR;
#define KTILE(bf, t, tp) do { \
    LDB(B0, bf, 0); SCHED; LDA(At, bf, 0); STA(SA(1 - bf, 1), 1, (t) + 1); \
    WAIT_L(8); BAR; WAIT_LB(B0); MMA(0, 0, B0); BAR; SCHED; \
    LDB(B1, bf, 1); STB(SB(bf, 0), cG, (t) + 2); CSUM(tp == tc0, B0); \
    BAR; WAIT_LB(B1); MMA(0, 1, B1); BAR; \
    LDA(At, bf, 1); STA(SA(bf, 0), 0, (t) + 2); \
    BAR; WAIT_L(0); MMA(1, 0, B0); BAR; SCHED; \
    STB(SB(bf, 1), cU, (t) + 2); CSUM(tp == tc1, B1); \
    WAIT_V(6); BAR; MMA(1, 1, B1); BAR; } while (0)
    for (int t = 0; t < 14; t += 2) {
      KTILE(0, t, t);
      STAMP(16 + t);
      if (t == E1) { TOUCH(Wd + (b * 512 + wid * 64 + lane_id()) * 64); SCHED; }
      if (t == 0 && tid == 0) FLAG_ST(OFF_F1 + b * 128);
      KTILE(1, t + 1, t);
      STAMP(17 + t);
      if (t == tc) {
        UNR for (int n = 0; n < 2; n++) { float v = cs[n];
          auto a = __builtin_amdgcn_permlane16_swap(__float_as_uint(v), __float_as_uint(v), false, false); v = __uint_as_float(a[0]) + __uint_as_float(a[1]);
          auto d = __builtin_amdgcn_permlane32_swap(__float_as_uint(v), __float_as_uint(v), false, false); cs[n] = __uint_as_float(d[0]) + __uint_as_float(d[1]); }
        const int l_ = lane_id();
        __builtin_amdgcn_raw_buffer_store_b64((u32x2){__float_as_uint((l_ & 16) ? cs[1] : cs[0]), epoch}, rsW, l_ < 32 ? OFF_CS + pn * 16384 + pm * 2048 + wid * 256 + l_ * 8 : WS_USED, 0, 16);
        SCHED; }
    }
    u32x2 rec, cq[4];
    const int ro = OFF_R + (pm * 256 + ((tid & 192) | ((tid & 1) << 5) | ((tid >> 1) & 31))) * 16, co = OFF_CS + pn * 16384 + wr * 8192 + (tid & 255) * 8;
#define EPI_LOAD do { BLD(rec, "buffer_load_dwordx2", ro, "sc1"); BLD(cq[0], "buffer_load_dwordx2", co, "sc1"); BLD(cq[1], "buffer_load_dwordx2", co, "offset:2048 sc1"); \
    BLD(cq[2], "buffer_load_dwordx2", co + 4096, "sc1"); BLD(cq[3], "buffer_load_dwordx2", co + 4096, "offset:2048 sc1"); } while (0)
#define EPI_WAIT asm volatile("s_waitcnt vmcnt(0)" : "+v"(rec), "+v"(cq[0]), "+v"(cq[1]), "+v"(cq[2]), "+v"(cq[3])::"memory")
    LDB(B0, 0, 0); LDA(At, 0, 0); STA(SA(1, 1), 1, 15); EPI_LOAD;
    BAR; WAIT_LB(B0); MMA(0, 0, B0); BAR;
    LDB(B1, 0, 1); BAR; WAIT_LB(B1); MMA(0, 1, B1); BAR;
    LDA(At, 0, 1); WAIT_V(9); BAR; WAIT_L(0); MMA(1, 0, B0); MMA(1, 1, B1); BAR;
    LDB(B0, 1, 0); LDA(At, 1, 0); WAIT_V(7); BAR; WAIT_LB(B0); MMA(0, 0, B0); BAR;
    LDB(B1, 1, 1); EPI_WAIT;
    for (unsigned sp = 0; sp < SPIN_CAP && !__all(rec[1] == epoch && cq[0][1] == epoch && cq[1][1] == epoch && cq[2][1] == epoch && cq[3][1] == epoch); ++sp) {
      __builtin_amdgcn_s_sleep(1); EPI_LOAD; EPI_WAIT; }
    unsigned f2;
    BLD(f2, "buffer_load_dword", wid == 0 ? OFF_F2 + rr * 128 + l31 * 4 : WS_USED, "sc1");
    { const float m = bf_lo(rec[0]), sd = bf_hi(rec[0]), r = __builtin_amdgcn_rcpf(sd);
      const float c0 = -2.302208198f;
      *(LAS f32x4*)(lds + L_EX + (tid & 255) * 16) = (f32x4){-m, c0 * r, c0 * 0.044715f * r * r * r, sd * sd};
      LDS1(L_EX + 4096 + tid * 4) = __float_as_uint((__uint_as_float(cq[0][0]) + __uint_as_float(cq[1][0])) + (__uint_as_float(cq[2][0]) + __uint_as_float(cq[3][0]))); }
    BAR; WAIT_LB(B1); MMA(0, 1, B1); BAR;
    LDA(At, 1, 1);
    BAR; WAIT_L(0); MMA(1, 0, B0); MMA(1, 1, B1);
    asm volatile("s_waitcnt vmcnt(0)" : "+v"(f2)::"memory");
    if (wid == 0) { LDS1(L_EX + 6144) = __all(f2 == epoch); WAIT_L(0); }
    BAR;
    if (wr == 0) BAR;
    STAMP(2);
    f32x4 rc[8], cg[2], cu[2];
    UNR for (int i = 0; i < 8; i++) rc[i] = *(LAS f32x4*)(lds + L_EX + ((i >> 2) * 128 + wr * 64 + (i & 3) * 16 + fr) * 16);
    UNR for (int n = 0; n < 2; n++) { const int o = L_EX + 4096 + (wc * 32 + n * 16 + fq * 4) * 4;
      cg[n] = *(LAS f32x4*)(lds + o) + *(LAS f32x4*)(lds + o + 1024); cu[n] = *(LAS f32x4*)(lds + o + 512) + *(LAS f32x4*)(lds + o + 1536); }
    p3_done = __builtin_amdgcn_readfirstlane(LDS1(L_EX + 6144)) != 0;
    u32x4 zz[4];
    P3_LOAD;
    SCHED; WAIT_L(0);
    G2_VOFF;
    const int hoff = OFF_H + ((pm * 16 + wr * 4) * 112 + pn * 4 + wc) * 1024 + lane * 16;
    UNR for (int i = 0; i < 8; i++) { float hv[8];
      UNR for (int j = 0; j < 8; j++) { const float gg = __builtin_fmaf(rc[i][0], cg[j >> 2][j & 3], acc[i >> 2][0][i & 3][j >> 2][j & 3]);
        const float uu = __builtin_fmaf(rc[i][0], cu[j >> 2][j & 3], acc[i >> 2][1][i & 3][j >> 2][j & 3]);
        const float ex = __builtin_amdgcn_exp2f(gg * __builtin_fmaf(gg * gg, rc[i][2], rc[i][1]));
        hv[j] = gg * uu * __builtin_amdgcn_rcpf(__builtin_fmaf(ex, rc[i][3], rc[i][3])); }
      ST_SC1(((u32x4){pk_bf16(hv[0], hv[1]), pk_bf16(hv[2], hv[3]), pk_bf16(hv[4], hv[5]), pk_bf16(hv[6], hv[7])}), hoff + ((i >> 2) * 8 + (i & 3)) * 112 * 1024);
      SCHED; STD1(i, 0); STD1(i, 1); SCHED; }
    XSTAMP(3);
    asm volatile("s_waitcnt vmcnt(24)" : "+v"(zz[0]), "+v"(zz[1]), "+v"(zz[2]), "+v"(zz[3])::"memory");
    if (p3_done) { P3_RUN(24); XSTAMP(4); WAIT_V(2); XPUT(4); } else WAIT_V(0);
    BAR;
    if (tid == 0) FLAG_ST(OFF_FH + (pm * 32 + pn) * 128);
    XPUT(3);
  } else {
    u32x4 FR[8], xc[8], xo[8];
#define FIX8(r, i) asm("" : "+v"(r[0]), "+v"(r[1]), "+v"(r[2]), "+v"(r[3]), "+v"(i[0]), "+v"(i[1]), "+v"(i[2]), "+v"(i[3]))
#define TWQ(dst, re, im, q, tw) do { float r_[4], i_[4]; \
      UNR for (int e = 0; e < 4; e++) { r_[e] = __builtin_fmaf(im[4 * (q) + e], TWH(tw[e], 1), 0.0f); i_[e] = __builtin_fmaf(im[4 * (q) + e], TWH(tw[e], 0), 0.0f); } SCHED; \
      UNR for (int e = 0; e < 4; e++) { r_[e] = __builtin_fmaf(re[4 * (q) + e], TWH(tw[e], 0), -r_[e]); i_[e] = __builtin_fmaf(re[4 * (q) + e], TWH(tw[e], 1), i_[e]); } \
      FIX8(r_, i_); UNR for (int e = 0; e < 4; e++) dst[e] = pk_bf16(r_[e], i_[e]); asm("" : "+v"(dst)); } while (0)
#define XBASE(c) (l31 * 64 + ((((wid & 1) + 2 * (c)) ^ ((l31 >> 1) & 3)) * 4 + ((wid >> 1) ^ (l31 >> 3))) * 4)
    {
    LANES;
    WAIT_ALL; BAR;
    if (tid == 0) FLAG_ST(OFF_F1 + b * 128);
    UNR for (int z = 0; z < 2; z++) {
      const float mult = (float)(z ? 2 * l31 + 1 : l31) * (1.0f / 64), b0 = (float)((8 * (wid & 3) + 4 * hh) * (2 - z) + 1 - z), ph = 0.25f * (float)(wid >> 2);
      u32x4 w;
      UNR for (int e = 0; e < 4; e++) { const float a = __builtin_fmaf(b0 + (float)(e * (2 - z)), mult, ph); w[e] = pk_bf16(cosr(a), cosr(a - 0.25f)); }
      LDS4((z ? L_O2 : L_O1) + wid * 1024 + lane * 16) = w; }
    TAB_TW(L_G1, 2, 1, (float)l31 * (1.0f / 2048));
    TAB_TW(L_TC, 1, 0, (float)(2 * l31 + 1) * (1.0f / 2048));
    UNR for (int i = 0; i < 4; i++) { FR[i] = LDS4(L_FR + i * 1024 + lane * 16);
      UNR for (int e = 0; e < 4; e++) FR[4 + i][e] = __builtin_amdgcn_alignbit(FR[i][e], FR[i][e], 16) ^ 0x8000u; }
    if (wid < 4) POLLS(OFF_F1 + tid * 128, 4);
    WAIT_L(0); BAR;
    if (g == 0 && tid == 0) FLAG_ST(OFF_E);
    STAMP(2);
    u32x4 sx[16], sw[16]; u32x2 rp[4], rn[4];
    const int so = g * SLAB + wid * 16384 + lane * 16;
    UNR for (int e = 0; e < 4; e++) { const int j = l31 + 32 * (8 * wid + 4 * hh + e), o = g ? WS_USED : OFF_R + 8;
      rp[e] = __builtin_amdgcn_raw_buffer_load_b64(rsW, o + j * 16, 0, 16); rn[e] = __builtin_amdgcn_raw_buffer_load_b64(rsW, o + ((2048 - j) & 2047) * 16, 0, 16); }
    UNR for (int i = 0; i < 16; i++) sx[i] = LD_SC1(OFF_X1 + so + i * 1024);
    UNR for (int i = 0; i < 16; i++) sw[i] = LD_SC1(OFF_WT + so + i * 1024);
    SCHED;
    if (g == 0) {
      u32x4 f1, f2;
      UNR for (int e = 0; e < 4; e++) {
        const float ar = bf_lo(rp[e][1]) + bf_lo(rn[e][1]), ai = bf_hi(rp[e][1]) - bf_hi(rn[e][1]), br = bf_lo(rp[e][0]) + bf_lo(rn[e][0]), bi = bf_hi(rp[e][0]) - bf_hi(rn[e][0]);
        f1[e] = pk_bf16(0.125f * (ar + br), 0.125f * (ai + bi)); f2[e] = pk_bf16(0.125f * (ar - br), 0.125f * (ai - bi)); }
      *(u32x4*)(ws + OFF_FF + tid * 16) = f1; *(u32x4*)(ws + OFF_FF + 8192 + tid * 16) = f2;
    }
    UNR for (int i = 0; i < 16; i++) LDS4(wid * 16384 + i * 1024 + lane * 16) = sx[i];
    WAIT_L(0); BAR;
    { const int x0 = XBASE(0) + hh * 16384;
      UNR for (int j = 0; j < 4; j++) { const int b0 = opq(x0 + (j >> 1) * 2048 + (j & 1) * 65536), b1 = opq(b0 ^ 32);
        UNR for (int i = 0; i < 8; i++) { xc[2 * j + (i >> 2)][i & 3] = LDS1(b0 + (i >> 2) * 32768 + (i & 3) * 4096); xo[2 * j + (i >> 2)][i & 3] = LDS1(b1 + (i >> 2) * 32768 + (i & 3) * 4096); } } }
    WAIT_L(0); BAR;
    STAMP(3);
    int wsb[8];
    UNR for (int e = 0; e < 8; e++) wsb[e] = opq(wid * 16384 + (lane >> 2) * 64 + (((e & 3) ^ ((lane >> 3) & 3)) * 4 + ((lane & 3) ^ ((lane >> 3) & 3) ^ hh ^ ((e >> 2) * 2))) * 4);
#define WPUT(i0) do { UNR for (int i = (i0); i < (i0) + 4; i++) UNR for (int e = 0; e < 4; e++) LDS1(wsb[(i & 1) * 4 + e] + i * 1024) = sw[i][e]; SCHED; } while (0)
    const int ex = opq(L_EX + lane * 16) - L_EX;
#define STAGE_A(x, W0) do { f32x16 r0 = ZERO16, i0 = ZERO16, r1 = ZERO16, i1 = ZERO16; u32x4 tw[4]; \
      UNR for (int k = 0; k < 4; k++) { tw[k] = LDS4(ex + (L_T1 + k * 1024)); r0 = MF32(x[k], FR[k], r0); i0 = MF32(x[k], FR[4 + k], i0); } SCHED; \
      WPUT(W0); \
      UNR for (int q = 0; q < 4; q++) { r1 = MF32(x[4 + q], FR[q], r1); SCHED; TWQ(x[q], r0, i0, q, tw[q]); i1 = MF32(x[4 + q], FR[4 + q], i1); tw[q] = LDS4(ex + (L_G1 + q * 1024)); SCHED; } \
      WPUT(W0 + 4); \
      UNR for (int q = 0; q < 4; q++) { TWQ(x[4 + q], r1, i1, q, tw[q]); SCHED; } } while (0)
    STAGE_A(xc, 0); STAGE_A(xo, 8);
    WAIT_V(0); WAIT_L(0); BAR;
    STAMP(6);
    }
#pragma unroll 1
    for (int c = 0; c < 2; c++) {
      LANES;
      const int ex = opq(L_EX + lane * 16) - L_EX, xb0 = XBASE(c);
      const int wb[2] = {opq(xb0 + hh * 8192), opq(xb0 + hh * 8192 + 65536)};
      f32x16 er = ZERO16, ei = ZERO16, xr, xi, cr = ZERO16, ci = ZERO16;
      u32x4 bq[4], w0, w1, fp[8], tw[4];
#define C1_LOAD do { UNR for (int k = 0; k < 2; k++) { fp[k] = LDS4(ex + (L_O2 + k * 1024)); fp[4 + k] = LDS4(ex + (L_O2 + 4096 + k * 1024)); } tw[0] = LDS4(ex + L_T1); } while (0)
      { u32x4 fo[8];
        UNR for (int k = 0; k < 8; k++) fo[k] = LDS4(ex + (L_O1 + k * 1024));
        UNR for (int k = 0; k < 4; k++) { er = MF32(FR[k], xc[k], er); ei = MF32(FR[4 + k], xc[k], ei); }
        SCHED;
        UNR for (int e = 0; e < 4; e++) { w0[e] = LDS1(wb[0] + e * 2048); w1[e] = LDS1(wb[1] + e * 2048); }
        SCHED;
        xr = MF32(fo[0], xc[4], er); xi = MF32(fo[4], xc[4], ei);
        UNR for (int k = 1; k < 4; k++) { xr = MF32(fo[k], xc[4 + k], xr); xi = MF32(fo[4 + k], xc[4 + k], xi); }
        SCHED; }
      if (g == 0 && wid == 0 && c == 0) {
        u32x4 ff[16];
        UNR for (int q = 0; q < 16; q++) ff[q] = *(const u32x4*)(ws + OFF_FF + ((q & 3) * 4 + (q >> 2)) * 1024 + lane * 16);
        UNR for (int i = 0; i < 16; i++) { er[i] = __builtin_fmaf(2.f, er[i], -xr[i]); ei[i] = __builtin_fmaf(2.f, ei[i], -xi[i]);
          LDS1(wb[0] + ((i & 3) + 8 * (i >> 2)) * 2048) = pk_bf16(xr[i], xi[i]); LDS1(wb[1] + ((i & 3) + 8 * (i >> 2)) * 2048) = pk_bf16(er[i], ei[i]); }
        const int lm = (32 - l31) & 31, m0 = (32 - l31) * 64 + (((lm >> 1) & 3) * 4 + (lm >> 3)) * 4 + (4 - 4 * hh) * 2048, mb[2] = {opq(m0 + 65536), opq(m0)};
        SCHED;
        UNR for (int q = 0; q < 4; q++) { u32x4 aq;
          UNR for (int e = 0; e < 4; e++) { float yr[2], yi[2];
            UNR for (int nu = 0; nu < 2; nu++) { const unsigned mm = LDS1(e + q + nu == 0 ? (lane == 0 ? 0 : mb[0] + 27 * 2048) : mb[nu] + (27 - e - 8 * q) * 2048);
              const float vr = nu ? er[4 * q + e] : xr[4 * q + e], vi = nu ? ei[4 * q + e] : xi[4 * q + e], mr = bf_lo(mm), mi = bf_hi(mm);
              const unsigned f1 = ff[4 * q + nu][e], f2 = ff[4 * q + 2 + nu][e];
              yr[nu] = vr * bf_lo(f1) - vi * bf_hi(f1) + mr * bf_lo(f2) + mi * bf_hi(f2);
              yi[nu] = -vr * bf_hi(f1) - vi * bf_lo(f1) - mr * bf_hi(f2) + mi * bf_lo(f2); }
            aq[e] = pk_bf16(yr[0] + yr[1], yi[0] + yi[1]); bq[q][e] = pk_bf16(yr[0] - yr[1], yi[0] - yi[1]); }
          cr = MF32(aq, FR[q], cr); ci = MF32(aq, FR[4 + q], ci); SCHED; }
        C1_LOAD;
      } else {
        UNR for (int q = 0; q < 4; q++) { float ar[4], ai[4], br[4], bi[4], vr[4], vi[4], wl[4], wh[4]; u32x4 aq; const u32x4 u0 = w0, u1 = w1;
          if (q < 3) UNR for (int e = 0; e < 4; e++) { w0[e] = LDS1(wb[0] + (e + 8 * q + 8) * 2048); w1[e] = LDS1(wb[1] + (e + 8 * q + 8) * 2048); }
          else C1_LOAD;
          UNR for (int e = 0; e < 4; e++) { wl[e] = bf_lo(u0[e]); wh[e] = bf_hi(u0[e]); } SCHED;
          UNR for (int e = 0; e < 4; e++) { br[e] = xi[4 * q + e] * wh[e]; bi[e] = xi[4 * q + e] * wl[e]; } FIX8(br, bi); SCHED;
          UNR for (int e = 0; e < 4; e++) { vr[e] = __builtin_fmaf(2.f, er[4 * q + e], -xr[4 * q + e]); vi[e] = __builtin_fmaf(2.f, ei[4 * q + e], -xi[4 * q + e]); } SCHED;
          UNR for (int e = 0; e < 4; e++) { br[e] = __builtin_fmaf(xr[4 * q + e], wl[e], -br[e]); bi[e] = __builtin_fmaf(-xr[4 * q + e], wh[e], -bi[e]); } SCHED;
          UNR for (int e = 0; e < 4; e++) { wl[e] = bf_lo(u1[e]); wh[e] = bf_hi(u1[e]); } SCHED;
          UNR for (int e = 0; e < 4; e++) { ar[e] = __builtin_fmaf(-vi[e], wh[e], br[e]); ai[e] = __builtin_fmaf(-vi[e], wl[e], bi[e]); } SCHED;
          UNR for (int e = 0; e < 4; e++) { ar[e] = __builtin_fmaf(vr[e], wl[e], ar[e]); ai[e] = __builtin_fmaf(-vr[e], wh[e], ai[e]); } SCHED;
          UNR for (int e = 0; e < 4; e++) { br[e] = __builtin_fmaf(2.f, br[e], -ar[e]); bi[e] = __builtin_fmaf(2.f, bi[e], -ai[e]); }
          FIX8(ar, ai); FIX8(br, bi); SCHED;
          UNR for (int e = 0; e < 4; e++) aq[e] = pk_bf16(ar[e], ai[e]); SCHED;
          UNR for (int e = 0; e < 2; e++) bq[q][e] = pk_bf16(br[e], bi[e]); SCHED;
          cr = MF32(aq, FR[q], cr); SCHED;
          UNR for (int e = 2; e < 4; e++) bq[q][e] = pk_bf16(br[e], bi[e]);
          asm("" : "+v"(bq[q])); SCHED;
          ci = MF32(aq, FR[4 + q], ci); SCHED; }
      }
      const int zb[2] = {opq(xb0 + hh * 16384), opq(xb0 + hh * 16384 + 65536)};
      f32x16 dr = ZERO16, di = ZERO16;
      er = ZERO16; ei = ZERO16; xr = ZERO16; xi = ZERO16;
      { UNR for (int k = 2; k < 4; k++) { fp[k] = LDS4(ex + (L_O2 + k * 1024)); fp[4 + k] = LDS4(ex + (L_O2 + 4096 + k * 1024)); }
        UNR for (int k = 1; k < 4; k++) tw[k] = LDS4(ex + (L_T1 + k * 1024));
        SCHED;
        UNR for (int k = 0; k < 2; k++) { er = MF32(bq[k], fp[k], er); ei = MF32(bq[k], fp[4 + k], ei); }
        SCHED;
        UNR for (int q = 0; q < 4; q++) { u32x4 up; TWQ(up, cr, ci, q, tw[q]); tw[q] = LDS4(ex + (L_TC + q * 1024)); SCHED;
          if (q & 1) ei = MF32(bq[2 + (q >> 1)], fp[6 + (q >> 1)], ei); else er = MF32(bq[2 + (q >> 1)], fp[2 + (q >> 1)], er);
          SCHED; dr = MF32(FR[q], up, dr); di = MF32(FR[4 + q], up, di); SCHED; }
        UNR for (int q = 0; q < 4; q++) { u32x4 up; TWQ(up, er, ei, q, tw[q]); SCHED;
          if (q) UNR for (int e = 0; e < 2; e++) LDS1(zb[(q - 1) >> 1] + ((q - 1) & 1) * 32768 + e * 4096) = pk_bf16(dr[4 * q - 4 + e], di[4 * q - 4 + e]);
          SCHED; xr = MF32(FR[q], up, xr); SCHED;
          if (q) UNR for (int e = 2; e < 4; e++) LDS1(zb[(q - 1) >> 1] + ((q - 1) & 1) * 32768 + e * 4096) = pk_bf16(dr[4 * q - 4 + e], di[4 * q - 4 + e]);
          SCHED; xi = MF32(FR[4 + q], up, xi); SCHED; }
        UNR for (int e = 0; e < 4; e++) LDS1(zb[1] + 32768 + e * 4096) = pk_bf16(dr[12 + e], di[12 + e]);
        UNR for (int k = 0; k < 8; k++) xc[k] = xo[k];
        SCHED;
        UNR for (int i = 0; i < 16; i++) LDS1(zb[i >> 3] + ((i >> 2) & 1) * 32768 + (i & 3) * 4096 + 2048) = pk_bf16(xr[i], xi[i]); }
    }
    STAMP(4);
    __builtin_amdgcn_s_setprio(0);
    {
    LANES;
    WAIT_L(0); BAR;
    XSTAMP(33);
    UNR for (int i = 0; i < 16; i++) ST_SC1(LDS4(wid * 16384 + i * 1024 + lane * 16), OFF_Z + g * SLAB + wid * 16384 + i * 1024 + lane * 16);
    XSTAMP(34);
    TAB_G1;
    WAIT_V(0); XSTAMP(35); WAIT_L(0); BAR;
    XPUT(33); XPUT(34); XPUT(35);
    if (tid < 32) FLAG_ST(OFF_F2 + tid * 128 + g * 4);
    STAMP(5);
    G2_PRELOAD;
    SCHED;
    }
  }
  STAMP(7);
  if (!p3_done) {
    LANES;
    if (wid == 0) POLL1(OFF_F2 + rr * 128 + l31 * 4);
    BAR;
    u32x4 zz[4];
    P3_LOAD; P3_RUN(0);
  }
  STAMP(8);

  {
    LANES;
    const int kg = wid >> 2, rg = wid & 3;
    if (wid == 0) { const int np = nkt >> 1; POLL1(OFF_FH + ((tm >> 1) * 32 + (kt0 >> 1) + (lane < np ? lane : 0)) * 128); }
    G2_VOFF;
    const int va0 = OFF_H + ((tm * 8 + rg * 2 + kg) * 112 + kt0 * 2 + kg) * 1024 + lane * 16, va1 = OFF_H + ((tm * 8 + rg * 2 + (kg ^ 1)) * 112 + kt0 * 2 + kg) * 1024 + lane * 16;
    int bo[8];
    { int q = fr >> 2, p = lane & 3; int row = 32 * kg + 4 * fq + q; int base = row * 256 + (((p >> 1) ^ ((row & 7) << 1)) * 16) + 8 * (p & 1);
      UNR for (int n = 0; n < 8; n++) bo[n] = base ^ (n * 32); }
    const int row = tm * 128 + (rg * 2 + kg) * 16 + fr, colo = tn * 128 + (fq & 1) * 16 + (fq >> 1) * 8;
    const int po = OFF_P + tile * 32768 + wid * 4096 + lane * 16;
    const int eo = half ? OFF_T + row * (D_ * 2) + colo * 2 : po, es = half ? 64 : 1024;
    f32x4 acc[2][8];
    UNR for (int a = 0; a < 2; a++) UNR for (int n = 0; n < 8; n++) acc[a][n] = (f32x4){0, 0, 0, 0};
    u32x4 Ar[4][2], ev[4]; u32x2 B0[8][2], B1[8][2];
    if (!isG) BAR;
    UNR for (int n = 0; n < 8; n++) { TRR(B0[n][0], bo[n], 0); TRR(B0[n][1], bo[n], 4096); }
    BAR;
    STAMP(9);
#define LDH1(sl, t, i) asm volatile("buffer_load_dwordx4 %0, %1, %2, %3 offen sc1" : "=v"(Ar[sl][i]) : "v"((i) ? va1 : va0), "s"(rsW), "s"((t) * 2048) : "memory")
#define LDE(u) asm volatile("buffer_load_dwordx4 %0, %1, %2, %3 offen sc1" : "=v"(ev[u]) : "v"(eo), "s"(rsW), "s"((u) * es) : "memory")
#define WAIT_LD(B) asm volatile("s_waitcnt lgkmcnt(0)" : "+v"(B[0][0]), "+v"(B[0][1]), "+v"(B[1][0]), "+v"(B[1][1]), "+v"(B[2][0]), "+v"(B[2][1]), "+v"(B[3][0]), "+v"(B[3][1]), \
   "+v"(B[4][0]), "+v"(B[4][1]), "+v"(B[5][0]), "+v"(B[5][1]), "+v"(B[6][0]), "+v"(B[6][1]), "+v"(B[7][0]), "+v"(B[7][1])::"memory")
#define WAIT_VA(n, sl) asm volatile("s_waitcnt vmcnt(" #n ")" : "+v"(Ar[sl][0]), "+v"(Ar[sl][1])::"memory")
#define FLIP do { UNR for (int n = 0; n < 8; n++) bo[n] ^= 65536; } while (0)
#define ITER(t, Bc, Bn, sl, so, VC, ISB, ISA, RD, DOBAR) do { WAIT_VA(VC, sl); WAIT_LD(Bc); if (DOBAR) BAR; SCHED; \
    UNR for (int n = 0; n < 8; n++) { \
      if (RD) { TRR(Bn[n][0], bo[n], so); TRR(Bn[n][1], bo[n], so + 4096); } \
      acc[n & 1][n] = MF16(CAT(Bc[n]), Ar[sl][n & 1], acc[n & 1][n]); SCHED; \
      acc[(n & 1) ^ 1][n] = MF16(CAT(Bc[n]), Ar[sl][(n & 1) ^ 1], acc[(n & 1) ^ 1][n]); SCHED; \
      if (ISB && n == 0) STD1((t) + 7, 0); if (ISB && n == 2) STD1((t) + 7, 1); \
      if (ISA == 1 && n == 4) LDH1(((sl) + 3) & 3, (t) + 3, 0); if (ISA == 1 && n == 6) LDH1(((sl) + 3) & 3, (t) + 3, 1); \
      if (ISA == 2 && n < 4) LDE(n); SCHED; } } while (0)
    LDH1(0, 0, 0); LDH1(0, 0, 1); LDH1(1, 1, 0); LDH1(1, 1, 1); LDH1(2, 2, 0); LDH1(2, 2, 1);
    ITER(0, B0, B1, 0, 16384, 4, 0, 1, 1, 1);
    if (tid == 0) FLAG_ST(OFF_F3 + b * 128);
    ITER(1, B1, B0, 1, 32768, 4, 1, 1, 1, 0);
    ITER(2, B0, B1, 2, 49152, 6, 1, 1, 1, 1); FLIP; ITER(3, B1, B0, 3, 0, 8, 1, 1, 1, 0);
    int t;
    for (t = 4; t < nkt - 8; t += 4) {
      ITER(t, B0, B1, 0, 16384, 8, 1, 1, 1, 1); ITER(t + 1, B1, B0, 1, 32768, 8, 1, 1, 1, 0);
      ITER(t + 2, B0, B1, 2, 49152, 8, 1, 1, 1, 1); FLIP; ITER(t + 3, B1, B0, 3, 0, 8, 1, 1, 1, 0); }
    ITER(t, B0, B1, 0, 16384, 8, 1, 1, 1, 1); ITER(t + 1, B1, B0, 1, 32768, 8, 0, 1, 1, 0);
    ITER(t + 2, B0, B1, 2, 49152, 6, 0, 1, 1, 1); FLIP; ITER(t + 3, B1, B0, 3, 0, 4, 0, 1, 1, 0);
    ITER(t + 4, B0, B1, 0, 16384, 4, 0, 1, 1, 1); ITER(t + 5, B1, B0, 1, 32768, 4, 0, 0, 1, 0);
    if (wid == 0) POLL1(half ? OFF_F3 + (16 * tm + fr) * 128 : OFF_FP + tile * 128);
    ITER(t + 6, B0, B1, 2, 49152, 2, 0, 2, 1, 1); ITER(t + 7, B1, B0, 3, 0, 4, 0, 0, 0, 1);
    STAMP(10);
    UNR for (int n = 0; n < 8; n++) *(LAS f32x4*)(lds + wid * 8192 + n * 1024 + lane * 16) = acc[1][n];
    WAIT_L(0); BAR;
    UNR for (int n = 0; n < 8; n++) acc[0][n] += *(LAS f32x4*)(lds + (wid ^ 4) * 8192 + n * 1024 + lane * 16);
    UNR for (int u = 0; u < 4; u++) UNR for (int j = 0; j < 4; j++) {
      auto sw = __builtin_amdgcn_permlane16_swap(__float_as_uint(acc[0][2 * u][j]), __float_as_uint(acc[0][2 * u + 1][j]), false, false);
      acc[0][2 * u][j] = __uint_as_float(sw[0]); acc[0][2 * u + 1][j] = __uint_as_float(sw[1]); }
    asm volatile("s_waitcnt vmcnt(0)" : "+v"(ev[0]), "+v"(ev[1]), "+v"(ev[2]), "+v"(ev[3])::"memory");
    UNR for (int u = 0; u < 4; u++) { const f32x4 lo = acc[0][2 * u], hi = acc[0][2 * u + 1];
      const u32x4 w = {pk_bf16(lo[0] + bf_lo(ev[u][0]), lo[1] + bf_hi(ev[u][0])), pk_bf16(lo[2] + bf_lo(ev[u][1]), lo[3] + bf_hi(ev[u][1])),
                       pk_bf16(hi[0] + bf_lo(ev[u][2]), hi[1] + bf_hi(ev[u][2])), pk_bf16(hi[2] + bf_lo(ev[u][3]), hi[3] + bf_hi(ev[u][3]))};
      if (half) ST_SC1(w, po + u * 1024); else *(u32x4*)(O + row * (D_ * 2) + (colo + 32 * u) * 2) = w; }
    if (half) { WAIT_V(0); BAR; if (tid == 0) FLAG_ST(OFF_FP + tile * 128); }
    STAMP(11);
  }
}

extern "C" void kernel_launch(void* const* d_in, const int* in_sizes, int n_in, void* d_out, int out_size, void* d_ws, size_t ws_size, hipStream_t stream) {
  (void)in_sizes; (void)n_in; (void)out_size; (void)ws_size;
  (void)hipFuncSetAttribute((const void*)fused_k, hipFuncAttributeMaxDynamicSharedMemorySize, LDS_BYTES);
  fused_k<<<256, 512, LDS_BYTES, stream>>>((const char*)d_in[0], (const char*)d_in[1], (const char*)d_in[2], (const char*)d_in[4],
                                           (const char*)d_in[5], (const char*)d_in[6], (char*)d_out, (char*)d_ws);
}
```

```cpp
#include <hip/hip_runtime.h>
#include <stdint.h>
#include <type_traits>
#define E1 6

#define LAS __attribute__((address_space(3)))
#define GAS __attribute__((address_space(1)))
typedef short bf16x8 __attribute__((ext_vector_type(8)));
typedef float f32x4 __attribute__((ext_vector_type(4)));
typedef float f32x16 __attribute__((ext_vector_type(16)));
typedef unsigned u32x4 __attribute__((ext_vector_type(4)));
typedef unsigned u32x2 __attribute__((ext_vector_type(2)));
typedef u32x4 u32x4u __attribute__((aligned(2)));
typedef __bf16 bf16x2v __attribute__((ext_vector_type(2)));
typedef _Float16 h16x2 __attribute__((ext_vector_type(2)));
typedef float f32x2 __attribute__((ext_vector_type(2)));

constexpr int S_ = 2048, D_ = 1024, F_ = 3584;
constexpr int OFF_E = 0, OFF_F2 = 4096, OFF_FP = 16384, OFF_R = 32768, OFF_FF = 65536, OFF_CS = 131072, OFF_F1 = 655360, OFF_F3 = OFF_F1 + 32768, OFF_FH = OFF_F3 + 32768, OFF_FB = OFF_FH + 64;
constexpr int SLAB = 135424;
constexpr int OFF_X1 = 1 << 20, OFF_WT = 6 << 20, OFF_Z = 11 << 20, OFF_T = 16 << 20, OFF_P = 20 << 20, OFF_H = 24 << 20, WS_USED = 40 << 20;
constexpr int L_EX = 131072, L_T1 = L_EX + 8192, L_G1 = L_EX + 12288, L_FR = L_EX + 16384, L_SC = L_EX + 24576, LDS_BYTES = 163840;
constexpr int L_RS = L_EX + 28672;
constexpr int L_O1 = L_EX, L_O2 = L_SC, L_TC = L_EX + 20480;
constexpr int NA = 32;
constexpr unsigned SPIN_CAP = 1u << 18;

__device__ __forceinline__ unsigned pk_bf16(float lo, float hi) { f32x2 v = {lo, hi}; return __builtin_bit_cast(unsigned, __builtin_convertvector(v, bf16x2v)); }
__device__ __forceinline__ unsigned pk_f16(float lo, float hi) { h16x2 v = {(_Float16)lo, (_Float16)hi}; return __builtin_bit_cast(unsigned, v); }
__device__ __forceinline__ float bf_lo(unsigned u) { return __uint_as_float(u << 16); }
__device__ __forceinline__ float bf_hi(unsigned u) { return __uint_as_float(u & 0xffff0000u); }
__device__ __forceinline__ float rbf(float f) { return bf_lo(pk_bf16(f, f)); }
__device__ __forceinline__ float cosr(float rev) { return __builtin_amdgcn_cosf(rev); }
__device__ __forceinline__ float dot2(unsigned a, unsigned b, float c) { return __builtin_amdgcn_fdot2_f32_bf16(__builtin_bit_cast(bf16x2v, a), __builtin_bit_cast(bf16x2v, b), c, false); }
__device__ __forceinline__ void cmul(float re, float im, unsigned t, float& r2, float& i2) {
  h16x2 tw = __builtin_bit_cast(h16x2, t);
  float a = __builtin_fmaf(im, (float)tw[1], 0.0f), b = __builtin_fmaf(im, (float)tw[0], 0.0f);
  r2 = __builtin_fmaf(re, (float)tw[0], -a); i2 = __builtin_fmaf(re, (float)tw[1], b);
  asm("" : "+v"(r2)); asm("" : "+v"(i2));
}
__device__ __forceinline__ float TWH(unsigned t, int h) { h16x2 tw = __builtin_bit_cast(h16x2, t); return (float)tw[h]; }
template <int CTRL> __device__ __forceinline__ float dppf(float v) { return __builtin_bit_cast(float, __builtin_amdgcn_update_dpp(0, __builtin_bit_cast(int, v), CTRL, 0xf, 0xf, true)); }
__device__ __forceinline__ float par_sum(float v) {
  v += dppf<0x4E>(v); v += dppf<0x124>(v); v += dppf<0x128>(v);
  auto a = __builtin_amdgcn_permlane16_swap(__float_as_uint(v), __float_as_uint(v), false, false); v = __uint_as_float(a[0]) + __uint_as_float(a[1]);
  auto b = __builtin_amdgcn_permlane32_swap(__float_as_uint(v), __float_as_uint(v), false, false); return __uint_as_float(b[0]) + __uint_as_float(b[1]);
}
__device__ __forceinline__ float rdl(float v, int l) { return __builtin_bit_cast(float, __builtin_amdgcn_readlane(__builtin_bit_cast(int, v), l)); }

__device__ __forceinline__ int opq(int v) { asm volatile("" : "+v"(v)); return v; }
__device__ __forceinline__ int lane_id() { int v; asm volatile("v_mbcnt_lo_u32_b32 %0, -1, 0\n\tv_mbcnt_hi_u32_b32 %0, -1, %0" : "=v"(v)); __builtin_assume(v >= 0 && v < 64); return v; }
#define LANES const int lane = lane_id(), tid = wid * 64 + lane, l31 = lane & 31, hh = lane >> 5, fr = lane & 15, fq = lane >> 4; (void)tid; (void)l31; (void)hh; (void)fr; (void)fq
#define BC8(x) __builtin_bit_cast(bf16x8, x)
#define MF32(a, b, c) __builtin_amdgcn_mfma_f32_32x32x16_bf16(BC8(a), BC8(b), c, 0, 0, 0)
#define MF16(a, b, c) __builtin_amdgcn_mfma_f32_16x16x32_bf16(BC8(a), BC8(b), c, 0, 0, 0)
#define MF16A(c, a, b) asm volatile("v_mfma_f32_16x16x32_bf16 %0, %1, %2, %0" : "+v"(c) : "v"(a), "v"(b))
#define WAIT_V(n) asm volatile("s_waitcnt vmcnt(" #n ")" ::: "memory")
#define WAIT_L(n) asm volatile("s_waitcnt lgkmcnt(" #n ")" ::: "memory")
#define WAIT_ALL do { __builtin_amdgcn_s_waitcnt(0x0070); asm volatile("" ::: "memory"); } while (0)
#define BAR __builtin_amdgcn_s_barrier()
#define BAR_IF(v, c, vc) asm volatile("s_cmp_lg_u32 %0, %1\n\ts_cbranch_scc1 1f\n\ts_waitcnt vmcnt(%2)\n\ts_barrier\n1:" :: "s"(v), "n"(c), "n"(vc) : "scc", "memory")
#define SCHED __builtin_amdgcn_sched_barrier(0)
#define UNR _Pragma("unroll")
#define LDS4(off) (*(LAS u32x4*)(lds + (off)))
#define LDS1(off) (*(LAS unsigned*)(lds + (off)))
#define ST_SC1(v, off) __builtin_amdgcn_raw_buffer_store_b128(v, rsW, off, 0, 16)
#define ST_SO(v, off, so) asm volatile("buffer_store_dwordx4 %0, %1, %2, %3 offen sc1\n\ts_nop 1" :: "v"(v), "v"(off), "s"(rsW), "s"(so) : "memory")
#define LD_SC1(off) __builtin_amdgcn_raw_buffer_load_b128(rsW, off, 0, 16)
#define FLAG_ST(off) __hip_atomic_store((GAS unsigned*)(ws + (off)), epoch, __ATOMIC_RELAXED, __HIP_MEMORY_SCOPE_AGENT)
#define FLAG_LD(off) __hip_atomic_load((GAS unsigned*)(ws + (off)), __ATOMIC_RELAXED, __HIP_MEMORY_SCOPE_AGENT)
#define POLLS(off, slp) do { const int po_ = (off); _Pragma("unroll 1") for (unsigned sp_ = 0; sp_ < SPIN_CAP && FLAG_LD(po_) != epoch; ++sp_) __builtin_amdgcn_s_sleep(slp); \
    __builtin_amdgcn_fence(__ATOMIC_ACQUIRE, "wavefront"); } while (0)
#define POLL1(off) POLLS(off, 1)
#define TRR(dst, base, off) asm volatile("ds_read_b64_tr_b16 %0, %1 offset:%2" : "=v"(dst) : "v"(base), "i"(off) : "memory")
#define CAT(x) __builtin_shufflevector(x[0], x[1], 0, 1, 2, 3)
#define STAMP(i)
#define XSTAMP(i)
#define XPUT(i)
#define LSTAMP(i)
#define TSTAMP(i)
#define LDUMP
#define NOST
#define KRV 8
#define PP0 0
#define ZPOS 3
#define SLP 2
#define T1POS 1
static const f32x16 ZERO16 = {0, 0, 0, 0, 0, 0, 0, 0, 0, 0, 0, 0, 0, 0, 0, 0};

__global__ __launch_bounds__(512, 2) void fused_k(const char* __restrict__ X, const char* __restrict__ Wr, const char* __restrict__ Wi,
                                                  const char* __restrict__ Wg, const char* __restrict__ Wu, const char* __restrict__ Wd,
                                                  char* __restrict__ O, char* ws) {
  extern __shared__ __attribute__((aligned(16))) char smem_[];
  LAS char* lds = (LAS char*)smem_;
  const int b = blockIdx.x, xg = b & 7, rr = b >> 3;
  const bool isG = rr < 28;
  const int g = xg * 4 + (rr & 3);
  const int wid = __builtin_amdgcn_readfirstlane(threadIdx.x >> 6);
  const auto rsW = __builtin_amdgcn_make_buffer_rsrc(ws, 0, WS_USED, 0x00020000);
  const char* pc0; asm volatile("s_getpc_b64 %0" : "=s"(pc0));
  STAMP(0);

  const int pm = (xg & 1) * 4 + (rr & 3), pn = (xg >> 1) * 7 + (rr >> 2);
  const int wr = wid >> 2, wc = wid & 3, kr = (xg & 1) * KRV;
  const auto cA = __builtin_amdgcn_make_buffer_rsrc((void*)(X + pm * 256 * (D_ * 2)), 0, 256 * D_ * 2, 0x00020000);
  const auto cG = __builtin_amdgcn_make_buffer_rsrc((void*)(Wg + pn * 256), 0, D_ * F_ * 2, 0x00020000);
  const auto cU = __builtin_amdgcn_make_buffer_rsrc((void*)(Wu + pn * 256), 0, D_ * F_ * 2, 0x00020000);
  const unsigned ldsw = wid * 1024;
#define G1_VOFF { int row = wid * 8 + (lane >> 3); voffA = row * (D_ * 2) + (((lane & 7) ^ (lane >> 3)) * 16); } \
  { int row = wid * 4 + fq; int swz = ((row & 3) | ((row >> 1) & 4)) << 1; voffB = row * (F_ * 2) + ((fr ^ swz) * 16); }
#define SA(bf, h) (((bf) * 2 + (h)) * 16384)
#define SB(bf, h) ((4 + (bf) * 2 + (h)) * 16384)
#define NTB 2
#define KR 8
#define DMA(rs, ldsoff, voff, soff) __builtin_amdgcn_raw_ptr_buffer_load_lds(rs, (LAS void*)(lds + (ldsoff)), 16, voff, soff, 0, 0)
#define DMB(rs, ldsoff, voff, soff) __builtin_amdgcn_raw_ptr_buffer_load_lds(rs, (LAS void*)(lds + (ldsoff)), 16, voff, soff, 0, NTB)
#define STA(bufoff, h, t) do { UNR for (int i_ = 0; i_ < 2; ++i_) DMA(cA, (bufoff) + ldsw + i_ * 8192, voffA, ((h) * 128 + i_ * 64) * (D_ * 2) + ((t) ^ kr) * 128); } while (0)
#define STB(bufoff, base, t) do { UNR for (int i_ = 0; i_ < 2; ++i_) DMB(base, (bufoff) + ldsw + i_ * 8192, voffB, (((t) ^ kr) * 64 + i_ * 32) * (F_ * 2)); } while (0)
  const int s = b * 8 + wid, sz = (s >> 1) & 15;
  const int tok = (s & ~63) | ((s & 31) << 1) | ((s >> 5) & 1);
#define TOUCH(p) __builtin_amdgcn_global_load_lds((const unsigned*)(p), (LAS unsigned*)(lds + L_SC + wid * 256), 4, 0, 0)
#define GLD(dst, ins, voff, sbase, mod) asm volatile(ins " %0, %1, %2 " mod : "=v"(dst) : "v"(voff), "s"(sbase) : "memory")
#define TAB_TW(dst, ca, cb, ml) do { const float m2 = (ml), t0 = (float)((ca) * (2 * (wid & 1) + 8 * (wid >> 1) + 4 * hh) + (cb)) * m2, t1 = t0 + (float)(ca) * m2; \
    *(LAS u32x2*)(lds + (dst) + (wid >> 1) * 1024 + lane * 16 + (wid & 1) * 8) = (u32x2){pk_f16(cosr(t0), cosr(t0 + 0.25f)), pk_f16(cosr(t1), cosr(t1 + 0.25f))}; } while (0)
#define TAB_G1 do { const int a = (l31 & 19) | ((l31 & 4) << 1) | ((l31 & 8) >> 1); \
    const float mult = (float)(wid < 4 ? a : l31) * (1.0f / 32), b0 = (float)(8 * (wid < 4 ? wid & 1 : wid & 3) + 4 * hh), ph = wid < 4 ? 0.25f * (float)(wid >> 1) : 0.f, sc = wid < 4 ? 0x1p-20f : 1.f; \
    u32x4 w; \
    UNR for (int e = 0; e < 4; e++) { const float t = __builtin_fmaf(b0 + (float)e, mult, ph); w[e] = pk_bf16(cosr(t) * sc, cosr(t - 0.25f) * sc); } \
    LDS4(L_G1 + wid * 1024 + lane * 16) = w; } while (0)
  unsigned epoch;
  u32x4 wt;
#define WSH(r) ((2 * (r)) & 12)
#define WLD(mat, ptr) do { const auto rw = __builtin_amdgcn_make_buffer_rsrc((void*)(ptr), 0, S_ * 1026, 0x00020000); \
    UNR for (int r = 0; r < 8; r++) DMA(rw, wid * 16896 + ((mat) * 8 + r) * 1056 + 16 - WSH(r), lane * 16, (g * 8 + wid) * 8208 + r * 1024); } while (0)
  f32x4 acc[2][2][4][2];
  unsigned voffA, voffB;
  int aoff0, aoff1, bo0, bo1;
  auto prologue = [&](auto gc) __attribute__((always_inline)) {
  constexpr bool isG = decltype(gc)::value;
  constexpr int L_ROW = isG ? L_EX : L_SC;
  LANES;
  LSTAMP(0);
  unsigned e0, w5r, w5i; u32x4 xa[2];
  { const int xo_ = l31 * 64 + hh * 16;
    GLD(xa[0], "global_load_dwordx4", xo_, X + tok * 2048, ""); GLD(xa[1], "global_load_dwordx4", xo_, X + tok * 2048, "offset:32"); }
  G1_VOFF;
  SCHED;
#define E0LD do { GLD(e0, "global_load_dword", 0, ws + OFF_E, "sc1"); \
    GLD(w5r, "global_load_ushort", (lane & 1) * 1024, Wr + s * 1026, ""); GLD(w5i, "global_load_ushort", (lane & 1) * 1024, Wi + s * 1026, ""); } while (0)
  if (isG) { STB(SB(0, 0), cG, 0); STA(SA(0, 0), 0, 0); } else { E0LD; const char* tp = ((lane & 8) ? Wi : Wr) + (g * 8 + wid) * 8208 + ((lane & 7) + 1) * 1024;
    asm volatile("global_load_dwordx4 %0, %1, off" : "=v"(wt) : "v"(tp) : "memory"); SCHED; WLD(0, Wr); }
  SCHED;
  LSTAMP(1);
  {
    const int e2 = ((l31 >> 1) & 1) + 2 * ((l31 >> 3) & 1), k2 = 4 * (2 * (l31 >> 4) + ((l31 >> 2) & 1)) + e2;
    const float mult = (float)(wid < 4 ? l31 : k2) * (1.0f / 32);
    const float ph = 0.25f * (float)(wid < 4 ? (wid >> 1) : (l31 & 1) - ((wid >> 1) & 1));
    const float b0 = (float)(16 * (wid & 1) + 4 * hh);
    float v[8];
    UNR for (int e = 0; e < 8; e++) v[e] = cosr(__builtin_fmaf(b0 + (float)((e & 3) + 8 * (e >> 2)), mult, ph));
    LDS4(L_ROW + wid * 1024 + lane * 16) = (u32x4){pk_bf16(v[0], v[1]), pk_bf16(v[2], v[3]), pk_bf16(v[4], v[5]), pk_bf16(v[6], v[7])};
    TAB_TW(L_T1, 1, 0, (float)l31 * (1.0f / 1024));
  }
  WAIT_L(0); BAR;
  LSTAMP(2);
  {
    u32x4 tb[12];
    UNR for (int i = 0; i < 12; i++) tb[i] = LDS4((i < 8 ? L_ROW : L_EX) + i * 1024 + lane * 16);
    TAB_G1;
    u32x4 idm[2];
    UNR for (int j = 0; j < 2; j++) { const int d = l31 - 16 * j - 8 * hh;
      UNR for (int p = 0; p < 4; p++) idm[j][p] = (d >> 1) == p ? (0x3f80u << (16 * (d & 1))) : 0u; }
    if (isG) UNR for (int a = 0; a < 32; a++) { acc[a >> 4][(a >> 3) & 1][(a >> 1) & 3][a & 1] = (f32x4){0, 0, 0, 0}; asm volatile("" : "+v"(acc[a >> 4][(a >> 3) & 1][(a >> 1) & 3][a & 1])); }
    if (isG) { aoff0 = opq((wr * 64 + fr) * 128 + ((fq ^ (fr & 7)) * 16)); aoff1 = opq(aoff0 ^ 64);
      const int q = fr >> 2, p = lane & 3, row = 8 * fq + q, swz = ((row & 3) | ((row >> 1) & 4)) << 1;
      bo0 = opq(row * 256 + (((2 * wc + (p >> 1)) ^ swz) * 16) + 8 * (p & 1) + 65536); bo1 = opq(bo0 ^ 128); }
    asm volatile("" : "+v"(idm[0]), "+v"(idm[1]));
    SCHED;
    LSTAMP(3);
    if (isG) { E0LD; STB(SB(0, 1), cU, 0); STA(SA(0, 1), 1, 0); } else WLD(1, Wi);
#define E4N 0
    SCHED;
    LSTAMP(4);
    asm volatile("s_waitcnt vmcnt(%2)" : "+v"(xa[0]), "+v"(xa[1]) : "n"((isG ? 11 : 20) + E4N) : "memory");
    LSTAMP(5);
    f32x16 xt = MF32(xa[0], idm[0], ZERO16); xt = MF32(xa[1], idm[1], xt);
    float s0 = 0.f, s1 = 0.f;
    UNR for (int j = 0; j < 2; j++) UNR for (int p = 0; p < 4; p++) { s0 = dot2(xa[j][p], 0x3f803f80u, s0); s1 = dot2(xa[j][p], 0xbf803f80u, s1); }
    const float sv = par_sum(((lane & 1) ? s1 : s0) + dppf<0xB1>((lane & 1) ? s0 : s1));
    const float mu = rdl(sv, 0) * (1.0f / D_), alt = rdl(sv, 1), m = rbf(mu);
    float q = 0.f;
    UNR for (int i = 0; i < 16; i++) { xt[i] -= m; q = __builtin_fmaf(xt[i], xt[i], q); }
    u32x4 df[2];
    UNR for (int i = 0; i < 8; i++) df[i >> 2][i & 3] = pk_bf16(xt[2 * i], xt[2 * i + 1]);
    f32x16 tr = MF32(df[0], tb[0], ZERO16), ti = MF32(df[0], tb[2], ZERO16);
    tr = MF32(df[1], tb[1], tr); ti = MF32(df[1], tb[3], ti);
    q = par_sum(q + dppf<0xB1>(q));
    const float var = fmaxf(q * (1.0f / D_) - (mu - m) * (mu - m), 0.f);
    const float sd = rbf(__builtin_amdgcn_sqrtf(rbf(rbf(var) + 1e-5f))), rs = __builtin_amdgcn_rcpf(sd);
    SCHED; if (T1POS == 0 && isG) { STB(SB(1, 0), cG, 1); STA(SA(1, 0), 0, 1); } SCHED;
    u32x4 br[2], bi[2];
    UNR for (int i = 0; i < 8; i++) { float r0, i0, r1, i1;
      cmul(tr[2 * i], ti[2 * i], tb[8 + (i >> 1)][(2 * i) & 3], r0, i0); cmul(tr[2 * i + 1], ti[2 * i + 1], tb[8 + (i >> 1)][(2 * i + 1) & 3], r1, i1);
      br[i >> 2][i & 3] = pk_bf16(r0, r1); bi[i >> 2][i & 3] = pk_bf16(i0, i1); }
    f32x16 xo = MF32(tb[4], br[0], ZERO16); xo = MF32(tb[6], bi[0], xo); xo = MF32(tb[5], br[1], xo); xo = MF32(tb[7], bi[1], xo);
    xo[1] = lane == 0 ? alt : xo[1];
    LSTAMP(6);
    const int so = SA(1, 1) + hh * 4096 + (l31 >> 4) * 512 + wid * 64 + ((fr ^ sz) * 4);
    const int go = OFF_X1 + (8 * hh + (l31 >> 4)) * SLAB + s * 64 + ((fr ^ sz) * 4);
    UNR for (int j = 0; j < 8; j++) { const unsigned v = pk_bf16(xo[2 * j] * rs, xo[2 * j + 1] * rs);
      if (isG) LDS1(so + (j >> 2) * 8192 + (j & 3) * 1024) = v; else __builtin_amdgcn_raw_buffer_store_b32(v, rsW, go, (16 * (j >> 2) + 2 * (j & 3)) * SLAB, 16); }
    SCHED;
    LSTAMP(7);
    if (T1POS == 1 && isG) { STB(SB(1, 0), cG, 1); STA(SA(1, 0), 0, 1); }
    LSTAMP(8);
    asm volatile("s_waitcnt vmcnt(%3)" : "+v"(e0), "+v"(w5r), "+v"(w5i) : "n"((isG ? (T1POS != 2 ? 8 : 4) : 24) + E4N) : "memory");
    LSTAMP(9);
    epoch = __builtin_amdgcn_readfirstlane(e0) * 0x9E3779B1u + 0x7F4A7C15u;
    const unsigned w0 = w5r | (w5i << 16), w5 = __builtin_amdgcn_readlane(w0, 1);
    if (isG) { if (lane == 0) LDS4(L_RS + wid * 16) = (u32x4){pk_bf16(m, sd), epoch, w5, w0}; }
    else { asm volatile("" : "+v"(wt)); ST_SC1(((u32x4){pk_bf16(m, sd), epoch, w5, w0}), lane == 0 ? OFF_R + s * 16 : WS_USED); }
    WAIT_L(0); BAR;
  }
  SCHED;
  };
#define PUBLISH(G) do { const int t_ = wid * 64 + lane_id(); \
    u32x4 s0_ = LDS4(SA(1, 1) + t_ * 16), s1_ = LDS4(SA(1, 1) + 8192 + t_ * 16), s2_ = LDS4(L_RS + (t_ & 7) * 16); SCHED; \
    if (G && T1POS == 2) { STB(SB(1, 0), cG, 1); STA(SA(1, 0), 0, 1); } \
    if (G) STB(SB(1, 1), cU, 1); \
    asm volatile("s_waitcnt lgkmcnt(0)" : "+v"(s0_), "+v"(s1_), "+v"(s2_)::"memory"); \
    if (G) STA(SA(1, 1), 1, 1); \
    const int o_ = NOST OFF_X1 + (t_ >> 5) * SLAB + b * 512 + (t_ & 31) * 16; SCHED; \
    ST_SC1(s0_, o_); ST_SC1(s1_, o_ + 16 * SLAB); ST_SC1(s2_, t_ < 8 ? OFF_R + b * 128 + t_ * 16 : WS_USED); SCHED; } while (0)
  const int half = xg & 1, tm = (xg >> 1) * 4 + (rr & 3), tn = rr >> 2, tile = tm * 8 + tn;
  const int kt0 = half ? NA : 0, nkt = half ? 56 - NA : NA;
  const auto cB = __builtin_amdgcn_make_buffer_rsrc((void*)(Wd + kt0 * 64 * (D_ * 2) + tn * 256), 0, F_ * D_ * 2, 0x00020000);
#define G2_VOFF unsigned voffD; { int row = wid * 4 + fq; voffD = row * (D_ * 2) + ((fr ^ ((row & 7) << 1)) * 16); }
  const int np = nkt >> 1;
#define KT(t) (2 * (t) - ((t) < np ? 0 : nkt - 1))
#define STD1(t, i) DMA(cB, ((t) & 7) * 16384 + ldsw + (i) * 8192, voffD, (KT(t) * 64 + (i) * 32) * (D_ * 2))
#define G2_PRELOAD do { G2_VOFF; UNR for (int t_ = 0; t_ < 8; t_++) { STD1(t_, 0); STD1(t_, 1); } } while (0)

#define BLD(dst, ins, voff, mod) asm volatile(ins " %0, %1, %2, 0 offen " mod : "=v"(dst) : "v"(voff), "s"(rsW) : "memory")
#define P3_ADDR const int zo_ = opq(OFF_Z + (8 * hh + (l31 >> 4)) * SLAB + s * 64 + ((fr ^ sz) * 4)), xo_ = opq(l31 * 64 + hh * 16)
#define P3_LOADZ(z_) asm volatile("buffer_load_dword %0, %1, %2, %3 offen sc1" : "=v"(zd[z_]) : "v"(zo_), "s"(rsW), "s"((16 * ((z_) >> 2) + 2 * ((z_) & 3)) * SLAB) : "memory")
#define P3_LOADX do { GLD(zx[0], "global_load_dwordx4", xo_, X + tok * 2048, ""); GLD(zx[1], "global_load_dwordx4", xo_, X + tok * 2048, "offset:32"); } while (0)
#define P3_WAIT(VC) asm volatile("s_waitcnt vmcnt(" #VC ")" : "+v"(zd[0]), "+v"(zd[1]), "+v"(zd[2]), "+v"(zd[3]), "+v"(zd[4]), "+v"(zd[5]), "+v"(zd[6]), "+v"(zd[7]), "+v"(zx[0]), "+v"(zx[1])::"memory")
#define HFLAG(o) __builtin_amdgcn_raw_buffer_store_b32(epoch, rsW, tid < 16 ? (o) + ((pm * 16 + tid) * 2 + (pn >> 4)) * 128 + (pn & 15) * 4 : WS_USED, 0, 16)
  bool p3_early = false;
  unsigned zd[8]; u32x4 zx[2];

  if (__builtin_expect(isG, 1)) {
    prologue(std::true_type{});
    LANES;
#define LDA(dst, bf, h) do { UNR for (int m = 0; m < 4; ++m) { dst[m][0] = LDS4(SA(bf, h) + aoff0 + m * 2048); dst[m][1] = LDS4(SA(bf, h) + aoff1 + m * 2048); } } while (0)
#define LDB(dst, bf, h) do { TRR(dst[0][0][0], bo0, SB(bf, h) - 65536); TRR(dst[0][0][1], bo0, SB(bf, h) - 65536 + 1024); \
    TRR(dst[1][0][0], bo1, SB(bf, h) - 65536); TRR(dst[1][0][1], bo1, SB(bf, h) - 65536 + 1024); \
    TRR(dst[0][1][0], bo0, SB(bf, h) - 65536 + 8192); TRR(dst[0][1][1], bo0, SB(bf, h) - 65536 + 9216); \
    TRR(dst[1][1][0], bo1, SB(bf, h) - 65536 + 8192); TRR(dst[1][1][1], bo1, SB(bf, h) - 65536 + 9216); } while (0)
#define WAIT_LB(B) asm volatile("s_waitcnt lgkmcnt(0)" : "+v"(B[0][0][0]), "+v"(B[0][0][1]), "+v"(B[0][1][0]), "+v"(B[0][1][1]), "+v"(B[1][0][0]), "+v"(B[1][0][1]), "+v"(B[1][1][0]), "+v"(B[1][1][1])::"memory")
#define MMA(ai, bj, Bt) do { __builtin_amdgcn_s_setprio(1); UNR for (int i = 0; i < 16; ++i) { const int m = i >> 2, n = ((i >> 1) ^ (i >> 2)) & 1, k = (i ^ (i >> 1)) & 1; \
    MF16A(acc[ai][bj][m][n], CAT(Bt[n][k]), At[m][k]); SCHED; } __builtin_amdgcn_s_setprio(0); } while (0)
#define CSUM(on, Bt) do { if (on) { UNR for (int i = 0; i < 16; i++) cs[i & 1] = dot2(Bt[i & 1][(i >> 1) & 1][(i >> 2) & 1][i >> 3], 0x3f803f80u, cs[i & 1]); } SCHED; } while (0)
    float cs[2] = {0.f, 0.f};
    const int tc = (rr & 3) * 2, tc0 = wr ? -1 : tc, tc1 = wr ? tc : -1;
    u32x4 At[4][2]; u32x2 B0[2][2][2], B1[2][2][2];
    TSTAMP(10);
    if (wr == 1) BAR;
#define PST(bf, tp, i)
#define KTILE(bf, t, tp) do { \
    LDB(B0, bf, 0); SCHED; LDA(At, bf, 0); \
    if (bf == 0 && tp == 0) { if (wr) { PUBLISH(1); WAIT_V(13); } } else { STA(SA(1 - bf, 1), 1, (t) + 1); WAIT_V(13); if (tp) WAIT_V(10); } \
    PST(bf, tp, 0); WAIT_L(8); BAR; WAIT_LB(B0); PST(bf, tp, 1); MMA(0, 0, B0); if (bf == 0 && tp + wr == 0) asm volatile("s_waitcnt vmcnt(%0)" :: "n"((T1POS == 2 ? 2 : 6) + E4N) : "memory"); BAR; SCHED; \
    LDB(B1, bf, 1); if (bf == 0 && tp + wr == 0) PUBLISH(1); STB(SB(bf, 0), cG, (t) + 2); CSUM(tp == tc0, B0); \
    WAIT_V(13); if (bf || tp) WAIT_V(10); \
    PST(bf, tp, 2); BAR; WAIT_LB(B1); PST(bf, tp, 3); MMA(0, 1, B1); BAR; \
    if (bf == 1 && tp + wid == 0 && lane_id() == 0) FLAG_ST(OFF_F1 + b * 128); \
    LDA(At, bf, 1); STA(SA(bf, 0), 0, (t) + 2); \
    PST(bf, tp, 4); BAR; WAIT_L(0); PST(bf, tp, 5); MMA(1, 0, B0); BAR; SCHED; \
    STB(SB(bf, 1), cU, (t) + 2); CSUM(tp == tc1, B1); \
    WAIT_V(13); if (bf || tp) WAIT_V(10); \
    PST(bf, tp, 6); BAR; PST(bf, tp, 7); MMA(1, 1, B1); BAR; } while (0)
#pragma unroll 1
    for (int t = 0; t < 14; t += 2) {
      asm volatile("" : "+s"(t));
      KTILE(0, t, t);
      TSTAMP(16 + t);
      if (t == E1) { TOUCH(Wd + (b * 512 + wid * 64 + lane_id()) * 64); SCHED; }
      KTILE(1, t + 1, t);
      TSTAMP(17 + t);
      if (t == tc) {
        UNR for (int n = 0; n < 2; n++) { float v = cs[n];
          auto a = __builtin_amdgcn_permlane16_swap(__float_as_uint(v), __float_as_uint(v), false, false); v = __uint_as_float(a[0]) + __uint_as_float(a[1]);
          auto d = __builtin_amdgcn_permlane32_swap(__float_as_uint(v), __float_as_uint(v), false, false); cs[n] = __uint_as_float(d[0]) + __uint_as_float(d[1]); }
        const int l_ = lane_id();
        __builtin_amdgcn_raw_buffer_store_b64((u32x2){__float_as_uint((l_ & 16) ? cs[1] : cs[0]), epoch}, rsW, l_ < 32 ? OFF_CS + pn * 16384 + pm * 2048 + wid * 256 + l_ * 8 : WS_USED, 0, 16);
        SCHED; }
    }
    u32x2 rec, cq[4];
    const int ro = OFF_R + (pm * 256 + ((tid & 192) | ((tid & 1) << 5) | ((tid >> 1) & 31))) * 16, co = OFF_CS + pn * 16384 + wr * 8192 + (tid & 255) * 8;
#define EPI_LOAD do { BLD(rec, "buffer_load_dwordx2", ro, "sc1"); BLD(cq[0], "buffer_load_dwordx2", co, "sc1"); BLD(cq[1], "buffer_load_dwordx2", co, "offset:2048 sc1"); \
    BLD(cq[2], "buffer_load_dwordx2", co + 4096, "sc1"); BLD(cq[3], "buffer_load_dwordx2", co + 4096, "offset:2048 sc1"); } while (0)
#define STD2(t) do { STD1(t, 0); STD1(t, 1); } while (0)
#define EPI_WAIT(n) asm volatile("s_waitcnt vmcnt(" #n ")" : "+v"(rec), "+v"(cq[0]), "+v"(cq[1]), "+v"(cq[2]), "+v"(cq[3])::"memory")
    LDB(B0, 0, 0); LDA(At, 0, 0); STA(SA(1, 1), 1, 15); EPI_LOAD;
    WAIT_V(15); BAR; WAIT_LB(B0); MMA(0, 0, B0); BAR;
    LDB(B1, 0, 1); WAIT_V(13); BAR; WAIT_LB(B1); MMA(0, 1, B1); BAR;
    G2_VOFF;
    LDA(At, 0, 1); SCHED; STD2(4); STD2(0); WAIT_V(13); BAR; WAIT_L(0); MMA(1, 0, B0); MMA(1, 1, B1); BAR;
    LDB(B0, 1, 0); LDA(At, 1, 0); SCHED; STD2(5); WAIT_V(13); BAR; WAIT_LB(B0); MMA(0, 0, B0); BAR;
    LDB(B1, 1, 1); EPI_WAIT(6);
    _Pragma("unroll 1") for (unsigned sp = 0; sp < SPIN_CAP && !__all(rec[1] == epoch && cq[0][1] == epoch && cq[1][1] == epoch && cq[2][1] == epoch && cq[3][1] == epoch); ++sp) {
      __builtin_amdgcn_s_sleep(1); EPI_LOAD; EPI_WAIT(0); }
    { const float m = bf_lo(rec[0]), sd = bf_hi(rec[0]), r = __builtin_amdgcn_rcpf(sd);
      const float c0 = -2.302208198f;
      *(LAS f32x4*)(lds + L_EX + (tid & 255) * 16) = (f32x4){-m, c0 * r, c0 * 0.044715f * r * r * r, sd * sd};
      LDS1(L_EX + 4096 + tid * 4) = __float_as_uint((__uint_as_float(cq[0][0]) + __uint_as_float(cq[1][0])) + (__uint_as_float(cq[2][0]) + __uint_as_float(cq[3][0]))); }
    SCHED; STD2(1);
    BAR; WAIT_LB(B1); MMA(0, 1, B1); BAR;
    LDA(At, 1, 1); SCHED; STD2(6); STD2(2);
    BAR; WAIT_L(0); MMA(1, 0, B0); MMA(1, 1, B1);
    BAR;
    XSTAMP(2); TSTAMP(40);
    f32x4 rc[8], cg[2], cu[2];
    UNR for (int i = 0; i < 8; i++) rc[i] = *(LAS f32x4*)(lds + L_EX + ((i >> 2) * 128 + wr * 64 + (i & 3) * 16 + fr) * 16);
    UNR for (int n = 0; n < 2; n++) { const int o = L_EX + 4096 + (wc * 32 + n * 16 + fq * 4) * 4;
      cg[n] = *(LAS f32x4*)(lds + o) + *(LAS f32x4*)(lds + o + 1024); cu[n] = *(LAS f32x4*)(lds + o + 512) + *(LAS f32x4*)(lds + o + 1536); }
    unsigned f2;
    BLD(f2, "buffer_load_dword", OFF_F2 + rr * 128 + l31 * 4, "sc1");
    const int hoff = opq(OFF_H + ((pm * 16 + wr * 4 + hh) * 112 + pn * 4 + (wc >> 1)) * 1024 + ((wc & 1) * 32 + l31) * 16);
    SCHED; WAIT_L(0); TSTAMP(41);
#define H2(v, j) ((f32x2){v[2 * ((j) & 1)], v[2 * ((j) & 1) + 1]})
#define R2(j, e) ((f32x2){rc[2 * i + ((j) >> 1)][e], rc[2 * i + ((j) >> 1)][e]})
#define AC(bj, j, n) acc[i >> 1][bj][2 * (i & 1) + ((j) >> 1)][n]
#define GEGLU(n) UNR for (int i = 0; i < 4; i++) { f32x2 gg[4], uu[4], ex[4]; u32x4 hv; \
      UNR for (int j = 0; j < 4; j++) gg[j] = __builtin_elementwise_fma(R2(j, 0), H2(cg[n], j), H2(AC(0, j, n), j)); \
      UNR for (int j = 0; j < 4; j++) ex[j] = gg[j] * gg[j]; \
      UNR for (int j = 0; j < 4; j++) ex[j] = __builtin_elementwise_fma(ex[j], R2(j, 2), R2(j, 1)); \
      UNR for (int j = 0; j < 4; j++) ex[j] = ex[j] * gg[j]; \
      SCHED; \
      UNR for (int j = 0; j < 4; j++) { ex[j][0] = __builtin_amdgcn_exp2f(ex[j][0]); ex[j][1] = __builtin_amdgcn_exp2f(ex[j][1]); uu[j] = __builtin_elementwise_fma(R2(j, 0), H2(cu[n], j), H2(AC(1, j, n), j)); \
        if (n && i < 2) P3_LOADZ((4 * i + j) & 7); if (n && i == 2 && j == 0) P3_LOADX; if (n && i == 3) STD1(7 - 4 * (j >> 1), j & 1); SCHED; } \
      UNR for (int j = 0; j < 4; j++) ex[j] = __builtin_elementwise_fma(ex[j], R2(j, 3), R2(j, 3)); \
      SCHED; \
      UNR for (int j = 0; j < 4; j++) { ex[j][0] = __builtin_amdgcn_rcpf(ex[j][0]); ex[j][1] = __builtin_amdgcn_rcpf(ex[j][1]); uu[j] = uu[j] * gg[j]; SCHED; } \
      UNR for (int j = 0; j < 4; j++) uu[j] = uu[j] * ex[j]; \
      UNR for (int j = 0; j < 4; j++) hv[j] = pk_bf16(uu[j][0], uu[j][1]); \
      UNR for (int j = 0; j < 2; j++) { auto sw = __builtin_amdgcn_permlane32_swap(hv[j], hv[2 + j], false, false); hv[j] = sw[0]; hv[2 + j] = sw[1]; } \
      ST_SO(hv, hoff, (((i >> 1) * 8 + (i & 1) * 2) * 112 + 2 * (n)) * 1024); \
      if (n == 1 && i + 1 == ZPOS) { BAR_IF(wr, 0, ZPOS < 3 ? 5 * ZPOS : ZPOS == 3 ? 13 : 18); HFLAG(OFF_FH); } \
      SCHED; TSTAMP(42 + 4 * (n) + i); }
    P3_ADDR;
    BAR_IF(wr, 0, 63);
    __builtin_amdgcn_s_setprio(2);
    GEGLU(0);
    __builtin_amdgcn_s_setprio(1);
    asm volatile("s_waitcnt vmcnt(4)" : "+v"(f2)::"memory");
    p3_early = __all(f2 == epoch);
    SCHED;
    BAR_IF(wr, 1, 0);
    TSTAMP(50);
    GEGLU(1);
    __builtin_amdgcn_s_setprio(0);
    P3_WAIT(6);
    XSTAMP(3); TSTAMP(52);
    XPUT(3); XPUT(2); LDUMP;
  } else {
    u32x4 FR[8], xc[8], xo[8];
#define FIX8(r, i) asm("" : "+v"(r[0]), "+v"(r[1]), "+v"(r[2]), "+v"(r[3]), "+v"(i[0]), "+v"(i[1]), "+v"(i[2]), "+v"(i[3]))
#define TWQ(dst, re, im, q, tw) do { float r_[4], i_[4]; \
      UNR for (int e = 0; e < 4; e++) { r_[e] = __builtin_fmaf(im[4 * (q) + e], TWH(tw[e], 1), 0.0f); i_[e] = __builtin_fmaf(im[4 * (q) + e], TWH(tw[e], 0), 0.0f); } SCHED; \
      UNR for (int e = 0; e < 4; e++) { r_[e] = __builtin_fmaf(re[4 * (q) + e], TWH(tw[e], 0), -r_[e]); i_[e] = __builtin_fmaf(re[4 * (q) + e], TWH(tw[e], 1), i_[e]); } \
      FIX8(r_, i_); UNR for (int e = 0; e < 4; e++) dst[e] = pk_bf16(r_[e], i_[e]); asm("" : "+v"(dst)); } while (0)
#define XBASE(c) (l31 * 64 + ((wid + 8 * (c)) ^ (l31 >> 1)) * 4)
    {
    prologue(std::false_type{});
    LANES;
    XSTAMP(36);
    {
      const int rb = wid * 16896 + (l31 >> 1) * 1056 + 16 + hh * 32 + (lane & 1) * 16, go = OFF_WT + hh * SLAB + (g * 8 + wid) * 512 + l31 * 16, sh = (lane & 2) * 8;
      WAIT_V(9);
      if (lane < 16) *(LAS u32x4u*)(lds + wid * 16896 + lane * 1056 + 1040 - WSH(lane & 7)) = wt;
      u32x4 v[16]; unsigned nx[16];
      UNR for (int j = 0; j < 16; j++) { v[j] = LDS4(rb + j * 64); nx[j] = LDS1(rb + j * 64 + 16); }
      SCHED;
      UNR for (int j = 0; j < 16; j++) {
        const u32x4 a = {__builtin_amdgcn_alignbit(v[j][1], v[j][0], sh), __builtin_amdgcn_alignbit(v[j][2], v[j][1], sh), __builtin_amdgcn_alignbit(v[j][3], v[j][2], sh), __builtin_amdgcn_alignbit(nx[j], v[j][3], sh)};
        ST_SO(a, go, 2 * j * SLAB); } }
    SCHED;
    XSTAMP(37);
    u32x4 w1;
    UNR for (int z = 0; z < 2; z++) {
      const float mult = (float)(z ? 2 * l31 + 1 : l31) * (1.0f / 64), b0 = (float)((8 * (wid & 3) + 4 * hh) * (2 - z) + 1 - z), ph = 0.25f * (float)(wid >> 2);
      u32x4 w;
      UNR for (int e = 0; e < 4; e++) { const float a = __builtin_fmaf(b0 + (float)(e * (2 - z)), mult, ph); w[e] = pk_bf16(cosr(a), cosr(a - 0.25f)); }
      if (z) LDS4(L_O2 + wid * 1024 + lane * 16) = w; else w1 = w; }
    TAB_TW(L_G1, 2, 1, (float)l31 * (1.0f / 2048));
    TAB_TW(L_TC, 1, 0, (float)(2 * l31 + 1) * (1.0f / 2048));
    UNR for (int i = 0; i < 4; i++) { FR[i] = LDS4(L_FR + i * 1024 + lane * 16);
      UNR for (int e = 0; e < 4; e++) FR[4 + i][e] = __builtin_amdgcn_alignbit(FR[i][e], FR[i][e], 16) ^ 0x8000u; }
    asm volatile("" : "+v"(w1));
    WAIT_ALL; BAR;
    if (tid == 0) FLAG_ST(OFF_F1 + b * 128);
    LDS4(L_O1 + wid * 1024 + lane * 16) = w1;
    STAMP(1);
    if (wid < 4) POLLS(OFF_F1 + tid * 128, 4);
    WAIT_L(0); BAR;
    if (g == 0 && tid == 0) FLAG_ST(OFF_E);
    STAMP(2);
    u32x4 sx[16], sw[16]; u32x2 rp[4], rn[4];
    const int so = g * SLAB + wid * 16384 + lane * 16;
    UNR for (int e = 0; e < 4; e++) { const int j = l31 + 32 * (8 * wid + 4 * hh + e), o = g ? WS_USED : OFF_R + 8;
      rp[e] = __builtin_amdgcn_raw_buffer_load_b64(rsW, o + j * 16, 0, 16); rn[e] = __builtin_amdgcn_raw_buffer_load_b64(rsW, o + ((2048 - j) & 2047) * 16, 0, 16); }
    UNR for (int i = 0; i < 16; i++) sx[i] = LD_SC1(OFF_X1 + so + i * 1024);
    SCHED;
    if (g == 0) {
      u32x4 f1, f2;
      UNR for (int e = 0; e < 4; e++) {
        const float ar = bf_lo(rp[e][1]) + bf_lo(rn[e][1]), ai = bf_hi(rp[e][1]) - bf_hi(rn[e][1]), br = bf_lo(rp[e][0]) + bf_lo(rn[e][0]), bi = bf_hi(rp[e][0]) - bf_hi(rn[e][0]);
        f1[e] = pk_bf16(0.125f * (ar + br), 0.125f * (ai + bi)); f2[e] = pk_bf16(0.125f * (ar - br), 0.125f * (ai - bi)); }
      *(u32x4*)(ws + OFF_FF + tid * 16) = f1; *(u32x4*)(ws + OFF_FF + 8192 + tid * 16) = f2;
    }
    { const int wo = OFF_WT + g * SLAB + wid * 16384 + fq * 512 + fr * 16;
      UNR for (int i = 0; i < 16; i++) { LDS4(wid * 16384 + i * 1024 + lane * 16) = sx[i]; SCHED; sw[i] = LD_SC1(wo + (i >> 1) * 2048 + (i & 1) * 256); SCHED; } }
    WAIT_L(0); BAR;
    { const int x0 = XBASE(0) + hh * 16384;
      UNR for (int j = 0; j < 4; j++) { const int b0 = opq(x0 + (j >> 1) * 2048 + (j & 1) * 65536), b1 = opq(b0 ^ 32);
        UNR for (int i = 0; i < 8; i++) { xc[2 * j + (i >> 2)][i & 3] = LDS1(b0 + (i >> 2) * 32768 + (i & 3) * 4096); xo[2 * j + (i >> 2)][i & 3] = LDS1(b1 + (i >> 2) * 32768 + (i & 3) * 4096); } } }
    WAIT_L(0); BAR;
    STAMP(3);
    int wsb[8];
    XPUT(36); XPUT(37);
    UNR for (int e = 0; e < 8; e++) wsb[e] = opq(wid * 16384 + (lane >> 1) * 64 + ((8 * (lane & 1) + e) ^ (lane >> 2)) * 4);
#define WPUT(i0) do { UNR for (int i = (i0); i < (i0) + 4; i += 2) { \
      UNR for (int e = 0; e < 8; e++) LDS1(wsb[e] + i * 1024) = __builtin_amdgcn_perm(sw[i + 1][e >> 1], sw[i][e >> 1], (e & 1) ? 0x07060302u : 0x05040100u); } SCHED; } while (0)
    const int ex = opq(L_EX + lane * 16) - L_EX;
#define STAGE_A(x, W0) do { f32x16 r0 = ZERO16, i0 = ZERO16, r1 = ZERO16, i1 = ZERO16; u32x4 tw[4]; \
      UNR for (int k = 0; k < 4; k++) { tw[k] = LDS4(ex + (L_T1 + k * 1024)); r0 = MF32(x[k], FR[k], r0); i0 = MF32(x[k], FR[4 + k], i0); } SCHED; \
      WPUT(W0); \
      UNR for (int q = 0; q < 4; q++) { r1 = MF32(x[4 + q], FR[q], r1); SCHED; TWQ(x[q], r0, i0, q, tw[q]); i1 = MF32(x[4 + q], FR[4 + q], i1); tw[q] = LDS4(ex + (L_G1 + q * 1024)); SCHED; } \
      WPUT(W0 + 4); \
      UNR for (int q = 0; q < 4; q++) { TWQ(x[4 + q], r1, i1, q, tw[q]); SCHED; } } while (0)
    STAGE_A(xc, 0); STAGE_A(xo, 8);
    WAIT_V(0); WAIT_L(0); BAR;
    STAMP(6);
    }
#pragma unroll 1
    for (int c = 0; c < 2; c++) {
      LANES;
      const int ex = opq(L_EX + lane * 16) - L_EX, xb0 = XBASE(c);
      const int wb[2] = {opq(xb0 + hh * 8192), opq(xb0 + hh * 8192 + 65536)};
      f32x16 er = ZERO16, ei = ZERO16, xr, xi, cr = ZERO16, ci = ZERO16;
      u32x4 bq[4], w0, w1, fp[8], tw[4];
#define C1_LOAD do { UNR for (int k = 0; k < 2; k++) { fp[k] = LDS4(ex + (L_O2 + k * 1024)); fp[4 + k] = LDS4(ex + (L_O2 + 4096 + k * 1024)); } tw[0] = LDS4(ex + L_T1); } while (0)
      { u32x4 fo[8];
        UNR for (int k = 0; k < 8; k++) fo[k] = LDS4(ex + (L_O1 + k * 1024));
        UNR for (int k = 0; k < 4; k++) { er = MF32(FR[k], xc[k], er); ei = MF32(FR[4 + k], xc[k], ei); }
        SCHED;
        UNR for (int e = 0; e < 4; e++) { w0[e] = LDS1(wb[0] + e * 2048); w1[e] = LDS1(wb[1] + e * 2048); }
        SCHED;
        xr = MF32(fo[0], xc[4], er); xi = MF32(fo[4], xc[4], ei);
        UNR for (int k = 1; k < 4; k++) { xr = MF32(fo[k], xc[4 + k], xr); xi = MF32(fo[4 + k], xc[4 + k], xi); }
        SCHED; }
      if (g == 0 && wid == 0 && c == 0) {
        u32x4 ff[16];
        UNR for (int q = 0; q < 16; q++) ff[q] = *(const u32x4*)(ws + OFF_FF + ((q & 3) * 4 + (q >> 2)) * 1024 + lane * 16);
        UNR for (int i = 0; i < 16; i++) { er[i] = __builtin_fmaf(2.f, er[i], -xr[i]); ei[i] = __builtin_fmaf(2.f, ei[i], -xi[i]);
          LDS1(wb[0] + ((i & 3) + 8 * (i >> 2)) * 2048) = pk_bf16(xr[i], xi[i]); LDS1(wb[1] + ((i & 3) + 8 * (i >> 2)) * 2048) = pk_bf16(er[i], ei[i]); }
        const int lm = (32 - l31) & 31, m0 = (32 - l31) * 64 + (lm >> 1) * 4 + (4 - 4 * hh) * 2048, mb[2] = {opq(m0 + 65536), opq(m0)};
        SCHED;
        UNR for (int q = 0; q < 4; q++) { u32x4 aq;
          UNR for (int e = 0; e < 4; e++) { float yr[2], yi[2];
            UNR for (int nu = 0; nu < 2; nu++) { const unsigned mm = LDS1(e + q + nu == 0 ? (lane == 0 ? 0 : mb[0] + 27 * 2048) : mb[nu] + (27 - e - 8 * q) * 2048);
              const float vr = nu ? er[4 * q + e] : xr[4 * q + e], vi = nu ? ei[4 * q + e] : xi[4 * q + e], mr = bf_lo(mm), mi = bf_hi(mm);
              const unsigned f1 = ff[4 * q + nu][e], f2 = ff[4 * q + 2 + nu][e];
              yr[nu] = vr * bf_lo(f1) - vi * bf_hi(f1) + mr * bf_lo(f2) + mi * bf_hi(f2);
              yi[nu] = -vr * bf_hi(f1) - vi * bf_lo(f1) - mr * bf_hi(f2) + mi * bf_lo(f2); }
            aq[e] = pk_bf16(yr[0] + yr[1], yi[0] + yi[1]); bq[q][e] = pk_bf16(yr[0] - yr[1], yi[0] - yi[1]); }
          cr = MF32(aq, FR[q], cr); ci = MF32(aq, FR[4 + q], ci); SCHED; }
        C1_LOAD;
      } else {
        UNR for (int q = 0; q < 4; q++) { float ar[4], ai[4], br[4], bi[4], vr[4], vi[4], wl[4], wh[4]; u32x4 aq; const u32x4 u0 = w0, u1 = w1;
          if (q < 3) UNR for (int e = 0; e < 4; e++) { w0[e] = LDS1(wb[0] + (e + 8 * q + 8) * 2048); w1[e] = LDS1(wb[1] + (e + 8 * q + 8) * 2048); }
          else C1_LOAD;
          UNR for (int e = 0; e < 4; e++) { wl[e] = bf_lo(u0[e]); wh[e] = bf_hi(u0[e]); } SCHED;
          UNR for (int e = 0; e < 4; e++) { br[e] = xi[4 * q + e] * wh[e]; bi[e] = xi[4 * q + e] * wl[e]; } FIX8(br, bi); SCHED;
          UNR for (int e = 0; e < 4; e++) { vr[e] = __builtin_fmaf(2.f, er[4 * q + e], -xr[4 * q + e]); vi[e] = __builtin_fmaf(2.f, ei[4 * q + e], -xi[4 * q + e]); } SCHED;
          UNR for (int e = 0; e < 4; e++) { br[e] = __builtin_fmaf(xr[4 * q + e], wl[e], -br[e]); bi[e] = __builtin_fmaf(-xr[4 * q + e], wh[e], -bi[e]); } SCHED;
          UNR for (int e = 0; e < 4; e++) { wl[e] = bf_lo(u1[e]); wh[e] = bf_hi(u1[e]); } SCHED;
          UNR for (int e = 0; e < 4; e++) { ar[e] = __builtin_fmaf(-vi[e], wh[e], br[e]); ai[e] = __builtin_fmaf(-vi[e], wl[e], bi[e]); } SCHED;
          UNR for (int e = 0; e < 4; e++) { ar[e] = __builtin_fmaf(vr[e], wl[e], ar[e]); ai[e] = __builtin_fmaf(-vr[e], wh[e], ai[e]); } SCHED;
          UNR for (int e = 0; e < 4; e++) { br[e] = __builtin_fmaf(2.f, br[e], -ar[e]); bi[e] = __builtin_fmaf(2.f, bi[e], -ai[e]); }
          FIX8(ar, ai); FIX8(br, bi); SCHED;
          UNR for (int e = 0; e < 4; e++) aq[e] = pk_bf16(ar[e], ai[e]); SCHED;
          UNR for (int e = 0; e < 2; e++) bq[q][e] = pk_bf16(br[e], bi[e]); SCHED;
          cr = MF32(aq, FR[q], cr); SCHED;
          UNR for (int e = 2; e < 4; e++) bq[q][e] = pk_bf16(br[e], bi[e]);
          asm("" : "+v"(bq[q])); SCHED;
          ci = MF32(aq, FR[4 + q], ci); SCHED; }
      }
      const int zb[2] = {opq(xb0 + hh * 16384), opq(xb0 + hh * 16384 + 65536)};
      f32x16 dr = ZERO16, di = ZERO16;
      er = ZERO16; ei = ZERO16; xr = ZERO16; xi = ZERO16;
      { UNR for (int k = 2; k < 4; k++) { fp[k] = LDS4(ex + (L_O2 + k * 1024)); fp[4 + k] = LDS4(ex + (L_O2 + 4096 + k * 1024)); }
        UNR for (int k = 1; k < 4; k++) tw[k] = LDS4(ex + (L_T1 + k * 1024));
        SCHED;
        UNR for (int k = 0; k < 2; k++) { er = MF32(bq[k], fp[k], er); ei = MF32(bq[k], fp[4 + k], ei); }
        SCHED;
        UNR for (int q = 0; q < 4; q++) { u32x4 up; TWQ(up, cr, ci, q, tw[q]); tw[q] = LDS4(ex + (L_TC + q * 1024)); SCHED;
          if (q & 1) ei = MF32(bq[2 + (q >> 1)], fp[6 + (q >> 1)], ei); else er = MF32(bq[2 + (q >> 1)], fp[2 + (q >> 1)], er);
          SCHED; dr = MF32(FR[q], up, dr); di = MF32(FR[4 + q], up, di); SCHED; }
        UNR for (int q = 0; q < 4; q++) { u32x4 up; TWQ(up, er, ei, q, tw[q]); SCHED;
          if (q) UNR for (int e = 0; e < 2; e++) LDS1(zb[(q - 1) >> 1] + ((q - 1) & 1) * 32768 + e * 4096) = pk_bf16(dr[4 * q - 4 + e], di[4 * q - 4 + e]);
          SCHED; xr = MF32(FR[q], up, xr); SCHED;
          if (q) UNR for (int e = 2; e < 4; e++) LDS1(zb[(q - 1) >> 1] + ((q - 1) & 1) * 32768 + e * 4096) = pk_bf16(dr[4 * q - 4 + e], di[4 * q - 4 + e]);
          SCHED; xi = MF32(FR[4 + q], up, xi); SCHED; }
        UNR for (int e = 0; e < 4; e++) LDS1(zb[1] + 32768 + e * 4096) = pk_bf16(dr[12 + e], di[12 + e]);
        UNR for (int k = 0; k < 8; k++) xc[k] = xo[k];
        SCHED;
        UNR for (int i = 0; i < 16; i++) LDS1(zb[i >> 3] + ((i >> 2) & 1) * 32768 + (i & 3) * 4096 + 2048) = pk_bf16(xr[i], xi[i]); }
    }
    STAMP(4);
    __builtin_amdgcn_s_setprio(0);
    {
    LANES;
    WAIT_L(0); BAR;
    XSTAMP(33);
    UNR for (int i = 0; i < 16; i++) ST_SC1(LDS4(wid * 16384 + i * 1024 + lane * 16), OFF_Z + g * SLAB + wid * 16384 + i * 1024 + lane * 16);
    XSTAMP(34);
    TAB_G1;
    WAIT_V(0); XSTAMP(35); WAIT_L(0); BAR;
    XPUT(33); XPUT(34); XPUT(35);
    if (tid < 32) FLAG_ST(OFF_F2 + tid * 128 + g * 4);
    STAMP(5);
    G2_PRELOAD;
    SCHED;
    }
  }
  STAMP(7);
  if (!p3_early) {
    LANES;
    POLL1(OFF_F2 + rr * 128 + l31 * 4);
    P3_ADDR; UNR for (int j = 0; j < 8; j++) P3_LOADZ(j); P3_LOADX; P3_WAIT(0);
    if (!isG) BAR;
  }
  STAMP(8);

  {
    LANES;
    const int kg = wid >> 2, rg = wid & 3;
    unsigned fb;
    const int fo = opq(OFF_FH + (tile * 2 + half) * 128 + (lane < np ? lane : 0) * 4);
    BLD(fb, "buffer_load_dword", fo, "sc1");
    u32x4 frr[4], up[4];
    { u32x4 g1[4], tw3[4];
      const int ex = opq(L_EX + lane * 16) - L_EX, ea = opq(L_EX + ((lane & 51) | ((lane & 4) << 1) | ((lane & 8) >> 1)) * 16) - L_EX;
      UNR for (int i = 0; i < 4; i++) g1[i] = LDS4(ex + (L_G1 + i * 1024));
      UNR for (int i = 0; i < 4; i++) tw3[i] = LDS4(ea + (L_T1 + i * 1024));
      UNR for (int i = 0; i < 4; i++) frr[i] = LDS4(ex + (L_FR + i * 1024));
      const float sgn = hh ? 0.f : ((lane & 1) ? 0x1p-20f : -0x1p-20f);
      const u32x4 zz[2] = {{zd[0], zd[1], zd[2], zd[3]}, {zd[4], zd[5], zd[6], zd[7]}};
      f32x16 re = MF32(zz[0], g1[0], ZERO16), im = MF32(zz[0], g1[2], ZERO16);
      re = MF32(zz[1], g1[1], re); im = MF32(zz[1], g1[3], im);
      const unsigned zq = __builtin_amdgcn_readlane(zd[0], 0);
      re[0] = __builtin_fmaf(sgn, bf_hi(zq), re[0]);
      UNR for (int i = 0; i < 16; i++) { float a_, c_; cmul(re[i], im[i], tw3[i >> 2][i & 3], a_, c_); up[i >> 2][i & 3] = pk_bf16(a_, c_); } }
    const int va0 = opq(OFF_H + ((tm * 8 + rg * 2 + kg) * 112 + kt0 * 2 + kg) * 1024 + lane * 16), va1 = opq(OFF_H + ((tm * 8 + rg * 2 + (kg ^ 1)) * 112 + kt0 * 2 + kg) * 1024 + lane * 16);
    u32x4 Ar[4][2], ev[4]; u32x2 B0[8][2], B1[8][2];
    asm volatile("s_waitcnt vmcnt(0)" : "+v"(fb), "+v"(up[0]), "+v"(up[1]), "+v"(up[2]), "+v"(up[3])::"memory");
#define SPIN(o) _Pragma("unroll 1") for (unsigned sp = 0; sp < SPIN_CAP && !__all(fb == epoch); ++sp) { \
      __builtin_amdgcn_s_sleep(SLP); BLD(fb, "buffer_load_dword", o, "sc1"); asm volatile("s_waitcnt vmcnt(0)" : "+v"(fb)::"memory"); }
    SPIN(fo);
    XSTAMP(9);
#define LDH1(sl, t, i) asm volatile("buffer_load_dwordx4 %0, %1, %2, %3 offen sc1" : "=v"(Ar[sl][i]) : "v"((i) ? va1 : va0), "s"(rsW), "s"(KT(t) * 2048) : "memory")
#define LDE(u) asm volatile("buffer_load_dwordx4 %0, %1, %2, %3 offen sc1" : "=v"(ev[u]) : "v"(eo), "s"(rsW), "s"((u) * es) : "memory")
#define WAIT_LD(B) asm volatile("s_waitcnt lgkmcnt(0)" : "+v"(B[0][0]), "+v"(B[0][1]), "+v"(B[1][0]), "+v"(B[1][1]), "+v"(B[2][0]), "+v"(B[2][1]), "+v"(B[3][0]), "+v"(B[3][1]), \
   "+v"(B[4][0]), "+v"(B[4][1]), "+v"(B[5][0]), "+v"(B[5][1]), "+v"(B[6][0]), "+v"(B[6][1]), "+v"(B[7][0]), "+v"(B[7][1])::"memory")
#define WAIT_VA(n, sl) asm volatile("s_waitcnt vmcnt(" #n ")" : "+v"(Ar[sl][0]), "+v"(Ar[sl][1])::"memory")
#define FLIP do { UNR for (int n = 0; n < 8; n++) bo[n] ^= 65536; } while (0)
#define ITER(t, Bc, Bn, sl, so, VC, ISB, ISA, RD, DOBAR) do { WAIT_VA(VC, sl); WAIT_LD(Bc); if (DOBAR) BAR; SCHED; \
    UNR for (int n = 0; n < 8; n++) { \
      if (RD) { TRR(Bn[n][0], bo[n], so); TRR(Bn[n][1], bo[n], so + 2048); } \
      acc[n & 1][n] = MF16(CAT(Bc[n]), Ar[sl][n & 1], acc[n & 1][n]); SCHED; \
      acc[(n & 1) ^ 1][n] = MF16(CAT(Bc[n]), Ar[sl][(n & 1) ^ 1], acc[(n & 1) ^ 1][n]); SCHED; \
      if (ISB && n == 0) STD1((t) + 7, 0); if (ISB && n == 2) STD1((t) + 7, 1); \
      if (ISA == 1 && n == 4) LDH1(((sl) + 3) & 3, (t) + 3, 0); if (ISA == 1 && n == 6) LDH1(((sl) + 3) & 3, (t) + 3, 1); \
      if (ISA == 2 && n < 4) LDE(n); SCHED; } } while (0)
    LDH1(0, 0, 0); LDH1(0, 0, 1); LDH1(1, 1, 0); LDH1(1, 1, 1); LDH1(2, 2, 0); LDH1(2, 2, 1);
    SCHED;
    { f32x16 y;
      UNR for (int i = 0; i < 8; i++) { y[2 * i] = bf_lo(zx[i >> 2][i & 3]); y[2 * i + 1] = bf_hi(zx[i >> 2][i & 3]); }
      UNR for (int k = 0; k < 4; k++) y = MF32(up[k], frr[k], y);
      u32x4 o[2];
      UNR for (int i = 0; i < 8; i++) o[i >> 2][i & 3] = pk_bf16(y[2 * i], y[2 * i + 1]);
      UNR for (int e = 0; e < 4; e++) { auto sw = __builtin_amdgcn_permlane16_swap(o[0][e], o[1][e], false, false); o[0][e] = sw[0]; o[1][e] = sw[1]; }
      const int to = OFF_T + tok * 2048 + fr * 64 + hh * 16 + (fq & 1) * 32;
      ST_SC1(o[0], to); ST_SC1(o[1], to + 1024); }
    G2_VOFF;
    int bo[8];
    { int q = fr >> 2, p = lane & 3; int row = 32 * kg + 16 * (fq >> 1) + 4 * (fq & 1) + q; int base = row * 256 + (((p >> 1) ^ ((row & 7) << 1)) * 16) + 8 * (p & 1);
      UNR for (int n = 0; n < 8; n++) bo[n] = base ^ (n * 32); }
    const int row = tm * 128 + (rg * 2 + kg) * 16 + fr, colo = tn * 128 + (fq & 1) * 16 + (fq >> 1) * 8;
    const int po = OFF_P + tile * 32768 + wid * 4096 + lane * 16;
    const int eo = half ? OFF_T + row * (D_ * 2) + colo * 2 : po, es = half ? 64 : 1024;
    f32x4 acc[2][8];
    UNR for (int a = 0; a < 2; a++) UNR for (int n = 0; n < 8; n++) acc[a][n] = (f32x4){0, 0, 0, 0};
    SCHED;
    UNR for (int n = 0; n < 8; n++) { TRR(B0[n][0], bo[n], 0); TRR(B0[n][1], bo[n], 2048); }
    ITER(0, B0, B1, 0, 16384, 6, 0, 1, 1, 1);
    XSTAMP(12);
    if (isG && wid == 0) HFLAG(OFF_FB);
    ITER(1, B1, B0, 1, 32768, 6, 1, 1, 1, 0);
    ITER(2, B0, B1, 2, 49152, 8, 1, 1, 1, 1); FLIP;
    BLD(fb, "buffer_load_dword", WS_USED, "sc1");
    ITER(3, B1, B0, 3, 0, 9, 1, 1, 1, 0);
    int t;
    for (t = 4; t < nkt - 8; t += 4) {
      ITER(t, B0, B1, 0, 16384, 9, 1, 1, 1, 1);
      asm volatile("s_waitcnt vmcnt(8)" : "+v"(fb)::"memory");
      if (t == np - 4) { SPIN(fo + (OFF_FB - OFF_FH)); if (tid == 0) FLAG_ST(OFF_F3 + b * 128); }
      ITER(t + 1, B1, B0, 1, 32768, 8, 1, 1, 1, 0);
      ITER(t + 2, B0, B1, 2, 49152, 8, 1, 1, 1, 1); FLIP;
      BLD(fb, "buffer_load_dword", t == np - 8 ? fo + (OFF_FB - OFF_FH) : WS_USED, "sc1");
      ITER(t + 3, B1, B0, 3, 0, 9, 1, 1, 1, 0); }
    ITER(t, B0, B1, 0, 16384, 8, 1, 1, 1, 1);
    asm volatile("s_waitcnt vmcnt(8)" : "+v"(fb)::"memory");
    ITER(t + 1, B1, B0, 1, 32768, 8, 0, 1, 1, 0);
    ITER(t + 2, B0, B1, 2, 49152, 6, 0, 1, 1, 1); FLIP; ITER(t + 3, B1, B0, 3, 0, 4, 0, 1, 1, 0);
    ITER(t + 4, B0, B1, 0, 16384, 4, 0, 1, 1, 1); ITER(t + 5, B1, B0, 1, 32768, 4, 0, 0, 1, 0);
    if (wid == 0) POLL1(half ? OFF_F3 + (16 * tm + fr) * 128 : OFF_FP + tile * 128);
    ITER(t + 6, B0, B1, 2, 49152, 2, 0, 2, 1, 1); ITER(t + 7, B1, B0, 3, 0, 4, 0, 0, 0, 1);
    STAMP(10); XPUT(9); XPUT(12);
    UNR for (int n = 0; n < 8; n++) *(LAS f32x4*)(lds + wid * 8192 + n * 1024 + lane * 16) = acc[1][n];
    WAIT_L(0); BAR;
    UNR for (int n = 0; n < 8; n++) acc[0][n] += *(LAS f32x4*)(lds + (wid ^ 4) * 8192 + n * 1024 + lane * 16);
    UNR for (int u = 0; u < 4; u++) UNR for (int j = 0; j < 4; j++) {
      auto sw = __builtin_amdgcn_permlane16_swap(__float_as_uint(acc[0][2 * u][j]), __float_as_uint(acc[0][2 * u + 1][j]), false, false);
      acc[0][2 * u][j] = __uint_as_float(sw[0]); acc[0][2 * u + 1][j] = __uint_as_float(sw[1]); }
    asm volatile("s_waitcnt vmcnt(0)" : "+v"(ev[0]), "+v"(ev[1]), "+v"(ev[2]), "+v"(ev[3])::"memory");
    UNR for (int u = 0; u < 4; u++) { const f32x4 lo = acc[0][2 * u], hi = acc[0][2 * u + 1];
      const u32x4 w = {pk_bf16(lo[0] + bf_lo(ev[u][0]), lo[1] + bf_hi(ev[u][0])), pk_bf16(lo[2] + bf_lo(ev[u][1]), lo[3] + bf_hi(ev[u][1])),
                       pk_bf16(hi[0] + bf_lo(ev[u][2]), hi[1] + bf_hi(ev[u][2])), pk_bf16(hi[2] + bf_lo(ev[u][3]), hi[3] + bf_hi(ev[u][3]))};
      if (half) ST_SC1(w, po + u * 1024); else *(u32x4*)(O + row * (D_ * 2) + (colo + 32 * u) * 2) = w; }
    if (half) { WAIT_V(0); BAR; if (tid == 0) FLAG_ST(OFF_FP + tile * 128); }
    STAMP(11);
  }
}

extern "C" void kernel_launch(void* const* d_in, const int* in_sizes, int n_in, void* d_out, int out_size, void* d_ws, size_t ws_size, hipStream_t stream) {
  (void)in_sizes; (void)n_in; (void)out_size; (void)ws_size;
  (void)hipFuncSetAttribute((const void*)fused_k, hipFuncAttributeMaxDynamicSharedMemorySize, LDS_BYTES);
  fused_k<<<256, 512, LDS_BYTES, stream>>>((const char*)d_in[0], (const char*)d_in[1], (const char*)d_in[2], (const char*)d_in[4],
                                           (const char*)d_in[5], (const char*)d_in[6], (char*)d_out, (char*)d_ws);
}
```

```cpp
#define ENC 9
#define ENCW 1
#include <hip/hip_runtime.h>
#include <stdint.h>
#define E1 6
#define E4

#define LAS __attribute__((address_space(3)))
#define GAS __attribute__((address_space(1)))
typedef short bf16x8 __attribute__((ext_vector_type(8)));
typedef float f32x4 __attribute__((ext_vector_type(4)));
typedef float f32x16 __attribute__((ext_vector_type(16)));
typedef unsigned u32x4 __attribute__((ext_vector_type(4)));
typedef unsigned u32x2 __attribute__((ext_vector_type(2)));
typedef u32x2 u32x2u __attribute__((aligned(2)));
typedef __bf16 bf16x2v __attribute__((ext_vector_type(2)));
typedef _Float16 h16x2 __attribute__((ext_vector_type(2)));
typedef float f32x2 __attribute__((ext_vector_type(2)));

constexpr int S_ = 2048, D_ = 1024, F_ = 3584;
constexpr int OFF_E = 0, OFF_F2 = 4096, OFF_FP = 16384, OFF_R = 32768, OFF_FF = 65536, OFF_CS = 131072, OFF_F1 = 655360, OFF_F3 = OFF_F1 + 32768, OFF_FH = OFF_F3 + 32768;
constexpr int SLAB = 135424;
constexpr int OFF_X1 = 1 << 20, OFF_WT = 6 << 20, OFF_Z = 11 << 20, OFF_T = 16 << 20, OFF_P = 20 << 20, OFF_H = 24 << 20, WS_USED = 40 << 20;
constexpr int L_EX = 131072, L_T1 = L_EX + 8192, L_G1 = L_EX + 12288, L_FR = L_EX + 16384, L_SC = L_EX + 24576, LDS_BYTES = 163840;
constexpr int L_O1 = L_EX, L_O2 = L_SC, L_TC = L_EX + 20480;
constexpr int NA = 32;
constexpr unsigned SPIN_CAP = 1u << 18;

__device__ __forceinline__ unsigned pk_bf16(float lo, float hi) { f32x2 v = {lo, hi}; return __builtin_bit_cast(unsigned, __builtin_convertvector(v, bf16x2v)); }
__device__ __forceinline__ unsigned pk_f16(float lo, float hi) { h16x2 v = {(_Float16)lo, (_Float16)hi}; return __builtin_bit_cast(unsigned, v); }
__device__ __forceinline__ float bf_lo(unsigned u) { return __uint_as_float(u << 16); }
__device__ __forceinline__ float bf_hi(unsigned u) { return __uint_as_float(u & 0xffff0000u); }
__device__ __forceinline__ float rbf(float f) { return bf_lo(pk_bf16(f, f)); }
__device__ __forceinline__ float cosr(float rev) { return __builtin_amdgcn_cosf(rev); }
__device__ __forceinline__ float dot2(unsigned a, unsigned b, float c) { return __builtin_amdgcn_fdot2_f32_bf16(__builtin_bit_cast(bf16x2v, a), __builtin_bit_cast(bf16x2v, b), c, false); }
__device__ __forceinline__ void cmul(float re, float im, unsigned t, float& r2, float& i2) {
  h16x2 tw = __builtin_bit_cast(h16x2, t);
  float a = __builtin_fmaf(im, (float)tw[1], 0.0f), b = __builtin_fmaf(im, (float)tw[0], 0.0f);
  r2 = __builtin_fmaf(re, (float)tw[0], -a); i2 = __builtin_fmaf(re, (float)tw[1], b);
  asm("" : "+v"(r2)); asm("" : "+v"(i2));
}
__device__ __forceinline__ float TWH(unsigned t, int h) { h16x2 tw = __builtin_bit_cast(h16x2, t); return (float)tw[h]; }
template <int CTRL> __device__ __forceinline__ float dppf(float v) { return __builtin_bit_cast(float, __builtin_amdgcn_update_dpp(0, __builtin_bit_cast(int, v), CTRL, 0xf, 0xf, true)); }
__device__ __forceinline__ float par_sum(float v) {
  v += dppf<0x4E>(v); v += dppf<0x124>(v); v += dppf<0x128>(v);
  auto a = __builtin_amdgcn_permlane16_swap(__float_as_uint(v), __float_as_uint(v), false, false); v = __uint_as_float(a[0]) + __uint_as_float(a[1]);
  auto b = __builtin_amdgcn_permlane32_swap(__float_as_uint(v), __float_as_uint(v), false, false); return __uint_as_float(b[0]) + __uint_as_float(b[1]);
}
__device__ __forceinline__ float rdl(float v, int l) { return __builtin_bit_cast(float, __builtin_amdgcn_readlane(__builtin_bit_cast(int, v), l)); }

__device__ __forceinline__ int opq(int v) { asm volatile("" : "+v"(v)); return v; }
__device__ __forceinline__ int lane_id() { int v; asm volatile("v_mbcnt_lo_u32_b32 %0, -1, 0\n\tv_mbcnt_hi_u32_b32 %0, -1, %0" : "=v"(v)); __builtin_assume(v >= 0 && v < 64); return v; }
#define LANES const int lane = lane_id(), tid = wid * 64 + lane, l31 = lane & 31, hh = lane >> 5, fr = lane & 15, fq = lane >> 4; (void)tid; (void)l31; (void)hh; (void)fr; (void)fq
#define BC8(x) __builtin_bit_cast(bf16x8, x)
#define MF32(a, b, c) __builtin_amdgcn_mfma_f32_32x32x16_bf16(BC8(a), BC8(b), c, 0, 0, 0)
#define MF16(a, b, c) __builtin_amdgcn_mfma_f32_16x16x32_bf16(BC8(a), BC8(b), c, 0, 0, 0)
#define WAIT_V(n) asm volatile("s_waitcnt vmcnt(" #n ")" ::: "memory")
#define WAIT_L(n) asm volatile("s_waitcnt lgkmcnt(" #n ")" ::: "memory")
#define WAIT_ALL do { __builtin_amdgcn_s_waitcnt(0x0070); asm volatile("" ::: "memory"); } while (0)
#define BAR __builtin_amdgcn_s_barrier()
#define SCHED __builtin_amdgcn_sched_barrier(0)
#define UNR _Pragma("unroll")
#define LDS4(off) (*(LAS u32x4*)(lds + (off)))
#define LDS1(off) (*(LAS unsigned*)(lds + (off)))
#define ST_SC1(v, off) __builtin_amdgcn_raw_buffer_store_b128(v, rsW, off, 0, 16)
#define LD_SC1(off) __builtin_amdgcn_raw_buffer_load_b128(rsW, off, 0, 16)
#define FLAG_ST(off) __hip_atomic_store((GAS unsigned*)(ws + (off)), epoch, __ATOMIC_RELAXED, __HIP_MEMORY_SCOPE_AGENT)
#define FLAG_LD(off) __hip_atomic_load((GAS unsigned*)(ws + (off)), __ATOMIC_RELAXED, __HIP_MEMORY_SCOPE_AGENT)
#define POLLS(off, slp) do { const int po_ = (off); _Pragma("unroll 1") for (unsigned sp_ = 0; sp_ < SPIN_CAP && FLAG_LD(po_) != epoch; ++sp_) __builtin_amdgcn_s_sleep(slp); \
    __builtin_amdgcn_fence(__ATOMIC_ACQUIRE, "wavefront"); } while (0)
#define POLL1(off) POLLS(off, 1)
#define TRR(dst, base, off) asm volatile("ds_read_b64_tr_b16 %0, %1 offset:%2" : "=v"(dst) : "v"(base), "i"(off) : "memory")
#define CAT(x) __builtin_shufflevector(x[0], x[1], 0, 1, 2, 3)
#define STAMP(i) do { if ((i) == 0 || (i) == ENC) { enc_ = __builtin_amdgcn_s_memrealtime(); __builtin_amdgcn_s_waitcnt(0xC07F); } } while (0)
#define XSTAMP(i)
#define XPUT(i)
static const f32x16 ZERO16 = {0, 0, 0, 0, 0, 0, 0, 0, 0, 0, 0, 0, 0, 0, 0, 0};

__global__ __launch_bounds__(512, 2) void fused_k(const char* __restrict__ X, const char* __restrict__ Wr, const char* __restrict__ Wi,
                                                  const char* __restrict__ Wg, const char* __restrict__ Wu, const char* __restrict__ Wd,
                                                  char* __restrict__ O, char* ws) {
  extern __shared__ __attribute__((aligned(16))) char smem_[];
  LAS char* lds = (LAS char*)smem_;
  const int b = blockIdx.x, xg = b & 7, rr = b >> 3;
  const bool isG = rr < 28;
  const int g = xg * 4 + (rr & 3);
  const int wid = __builtin_amdgcn_readfirstlane(threadIdx.x >> 6);
  const auto rsW = __builtin_amdgcn_make_buffer_rsrc(ws, 0, WS_USED, 0x00020000);
  unsigned long long enc_; STAMP(0);

  const int pm = (xg & 1) * 4 + (rr & 3), pn = (xg >> 1) * 7 + (rr >> 2);
  const int wr = wid >> 2, wc = wid & 3, kr = isG ? (xg & 1) * 8 : 0;
  const int g28 = g < 28 ? g : g - 28;
  const char* cA = isG ? X + pm * 256 * (D_ * 2) : Wd + (g >> 3) * 256 * (D_ * 2) + (g & 7) * 256;
  const char* cG = isG ? Wg + pn * 256 : Wd + 384 * (F_ * 2) + g28 * 256;
  const char* cU = isG ? Wu + pn * 256 : Wd + 512 * (F_ * 2) + g28 * 256;
  const unsigned ldsw = wid * 1024;
#define G1_VOFF unsigned voffA, voffB; { int row = wid * 8 + (lane >> 3); voffA = row * (D_ * 2) + (((lane & 7) ^ (lane >> 3)) * 16); } \
  { int row = wid * 4 + fq; int swz = ((row & 3) | ((row >> 1) & 4)) << 1; voffB = row * (F_ * 2) + ((fr ^ swz) * 16); }
#define SA(bf, h) (((bf) * 2 + (h)) * 16384)
#define SB(bf, h) ((4 + (bf) * 2 + (h)) * 16384)
#define STA(bufoff, h, t) do { const char* g_ = cA + (h) * 128 * (D_ * 2) + ((t) ^ kr) * 128; UNR for (int i_ = 0; i_ < 2; ++i_) \
    __builtin_amdgcn_global_load_lds((const unsigned*)(g_ + i_ * 64 * (D_ * 2) + voffA), (LAS unsigned*)(lds + (bufoff) + ldsw + i_ * 8192), 16, 0, 0); } while (0)
#define STB(bufoff, base, t) do { const char* g_ = (base) + ((t) ^ kr) * (64 * F_ * 2); UNR for (int i_ = 0; i_ < 2; ++i_) \
    __builtin_amdgcn_global_load_lds((const unsigned*)(g_ + i_ * 32 * (F_ * 2) + voffB), (LAS unsigned*)(lds + (bufoff) + ldsw + i_ * 8192), 16, 0, 0); } while (0)
  const int s = b * 8 + wid, sq = (wid >> 1) & 3, se = b & 3;
  const int tok = (s & ~63) | ((s & 31) << 1) | ((s >> 5) & 1);
#define WNT ""
#define TOUCH(p) __builtin_amdgcn_global_load_lds((const unsigned*)(p), (LAS unsigned*)(lds + L_SC + wid * 256), 4, 0, 0)
#define GLD(dst, ins, voff, sbase, mod) asm volatile(ins " %0, %1, %2 " mod : "=v"(dst) : "v"(voff), "s"(sbase) : "memory")
#define TAB_TW(dst, ca, cb, ml) do { const float m2 = (ml), t0 = (float)((ca) * (2 * (wid & 1) + 8 * (wid >> 1) + 4 * hh) + (cb)) * m2, t1 = t0 + (float)(ca) * m2; \
    *(LAS u32x2*)(lds + (dst) + (wid >> 1) * 1024 + lane * 16 + (wid & 1) * 8) = (u32x2){pk_f16(cosr(t0), cosr(t0 + 0.25f)), pk_f16(cosr(t1), cosr(t1 + 0.25f))}; } while (0)
#define TAB_G1 do { const int a = (l31 & 19) | ((l31 & 4) << 1) | ((l31 & 8) >> 1), sx_ = wid < 4 ? se : 0; \
    const float mult = (float)(wid < 4 ? a : l31) * (1.0f / 32), b0 = (float)(8 * (wid < 4 ? wid & 1 : wid & 3) + 4 * hh), ph = wid < 4 ? 0.25f * (float)(wid >> 1) : 0.f; \
    u32x4 w; \
    UNR for (int e = 0; e < 4; e++) { const float t = __builtin_fmaf(b0 + (float)(e ^ sx_), mult, ph); w[e] = pk_bf16(cosr(t), cosr(t - 0.25f)); } \
    LDS4(L_G1 + wid * 1024 + lane * 16) = w; } while (0)
  unsigned epoch;
  {
  LANES;
  unsigned e0, w5r, w5i; u32x4 xa[2]; u32x2 wrv[2], wiv[2];
  { const int xo_ = l31 * 64 + hh * 16;
    GLD(xa[0], "global_load_dwordx4", xo_, X + tok * 2048, ""); GLD(xa[1], "global_load_dwordx4", xo_, X + tok * 2048, "offset:32");
    GLD(e0, "global_load_dword", 0, ws + OFF_E, "sc1"); }
  TOUCH(ws + (wid * 5 + (lane < 5 ? lane : 0)) * (1 << 20) + 4096);
#define E4N 16
  G1_VOFF;
  SCHED;
  STB(SB(0, 0), cG, 0); STA(SA(0, 0), 0, 0); STB(SB(0, 1), cU, 0); STA(SA(0, 1), 1, 0);
  SCHED;
  {
    const int e2 = ((l31 >> 1) & 1) + 2 * ((l31 >> 3) & 1), k2 = 4 * (2 * (l31 >> 4) + ((l31 >> 2) & 1)) + (e2 ^ se);
    const float mult = (float)(wid < 4 ? l31 : k2) * (1.0f / 32);
    const float ph = 0.25f * (float)(wid < 4 ? (wid >> 1) : (l31 & 1) - ((wid >> 1) & 1));
    const float b0 = (float)(16 * (wid & 1) + 4 * hh);
    float v[8];
    UNR for (int e = 0; e < 8; e++) v[e] = cosr(__builtin_fmaf(b0 + (float)((e & 3) + 8 * (e >> 2)), mult, ph));
    LDS4(L_EX + wid * 1024 + lane * 16) = (u32x4){pk_bf16(v[0], v[1]), pk_bf16(v[2], v[3]), pk_bf16(v[4], v[5]), pk_bf16(v[6], v[7])};
    TAB_TW(L_T1, 1, 0, (float)l31 * (1.0f / 1024));
  }
  WAIT_L(0); BAR;
  XSTAMP(12);
  { const int wo_ = ((lane >> 2) & 7) * 8 + (lane & 3) * 64 + hh * 256;
    GLD(wrv[0], "global_load_dwordx2", wo_, Wr + s * 1026, WNT); GLD(wiv[0], "global_load_dwordx2", wo_, Wi + s * 1026, WNT);
    GLD(wrv[1], "global_load_dwordx2", wo_, Wr + s * 1026, "offset:512 " WNT); GLD(wiv[1], "global_load_dwordx2", wo_, Wi + s * 1026, "offset:512 " WNT);
    GLD(w5r, "global_load_ushort", 0, Wr + s * 1026, "offset:1024"); GLD(w5i, "global_load_ushort", 0, Wi + s * 1026, "offset:1024"); }
  {
    u32x4 tb[12];
    UNR for (int i = 0; i < 12; i++) tb[i] = LDS4(L_EX + i * 1024 + lane * 16);
    TAB_G1;
    u32x4 idm[2];
    UNR for (int j = 0; j < 2; j++) { const int d = l31 - 16 * j - 8 * hh;
      UNR for (int p = 0; p < 4; p++) idm[j][p] = (d >> 1) == p ? (0x3f80u << (16 * (d & 1))) : 0u; }
    asm volatile("s_waitcnt vmcnt(%2)" : "+v"(xa[0]), "+v"(xa[1]) : "n"(E4N) : "memory");
    XSTAMP(13);
    f32x16 xt = MF32(xa[0], idm[0], ZERO16); xt = MF32(xa[1], idm[1], xt);
    float s0 = 0.f, s1 = 0.f;
    UNR for (int j = 0; j < 2; j++) UNR for (int p = 0; p < 4; p++) { s0 = dot2(xa[j][p], 0x3f803f80u, s0); s1 = dot2(xa[j][p], 0xbf803f80u, s1); }
    const float sv = par_sum(((lane & 1) ? s1 : s0) + dppf<0xB1>((lane & 1) ? s0 : s1));
    const float mu = rdl(sv, 0) * (1.0f / D_), alt = rdl(sv, 1), m = rbf(mu);
    float q = 0.f;
    UNR for (int i = 0; i < 16; i++) { xt[i] -= m; q = __builtin_fmaf(xt[i], xt[i], q); }
    u32x4 df[2];
    UNR for (int i = 0; i < 8; i++) df[i >> 2][i & 3] = pk_bf16(xt[2 * i], xt[2 * i + 1]);
    f32x16 tr = MF32(df[0], tb[0], ZERO16), ti = MF32(df[0], tb[2], ZERO16);
    tr = MF32(df[1], tb[1], tr); ti = MF32(df[1], tb[3], ti);
    q = par_sum(q + dppf<0xB1>(q));
    const float var = fmaxf(q * (1.0f / D_) - (mu - m) * (mu - m), 0.f);
    const float sd = rbf(__builtin_amdgcn_sqrtf(rbf(rbf(var) + 1e-5f))), rs = __builtin_amdgcn_rcpf(sd);
    SCHED; STB(SB(1, 0), cG, 1); STA(SA(1, 0), 0, 1); STB(SB(1, 1), cU, 1); SCHED;
    u32x4 br[2], bi[2];
    UNR for (int i = 0; i < 8; i++) { float r0, i0, r1, i1;
      cmul(tr[2 * i], ti[2 * i], tb[8 + (i >> 1)][(2 * i) & 3], r0, i0); cmul(tr[2 * i + 1], ti[2 * i + 1], tb[8 + (i >> 1)][(2 * i + 1) & 3], r1, i1);
      br[i >> 2][i & 3] = pk_bf16(r0, r1); bi[i >> 2][i & 3] = pk_bf16(i0, i1); }
    f32x16 xo = MF32(tb[4], br[0], ZERO16); xo = MF32(tb[6], bi[0], xo); xo = MF32(tb[5], br[1], xo); xo = MF32(tb[7], bi[1], xo);
    UNR for (int e = 0; e < 4; e++) xo[2 * e + 1] = (lane == 0 && se == e) ? alt : xo[2 * e + 1];
    XSTAMP(15);
    asm volatile("s_waitcnt vmcnt(6)" : "+v"(e0), "+v"(wrv[0]), "+v"(wrv[1]), "+v"(wiv[0]), "+v"(wiv[1]), "+v"(w5r), "+v"(w5i)::"memory");
    XSTAMP(14);
    epoch = __builtin_amdgcn_readfirstlane(e0) * 0x9E3779B1u + 0x7F4A7C15u;
    const int xoff = OFF_X1 + ((l31 >> 2) * 4 + hh) * SLAB + s * 64 + (((lane & 3) ^ sq) * 16);
    UNR for (int u = 0; u < 2; u++) { u32x4 w;
      UNR for (int e = 0; e < 4; e++) w[e] = pk_bf16(xo[8 * u + 2 * e] * rs, xo[8 * u + 2 * e + 1] * rs);
      ST_SC1(w, xoff + 2 * u * SLAB); }
    UNR for (int u = 0; u < 2; u++) { u32x4 w;
      UNR for (int e = 0; e < 4; e++) w[e] = __builtin_amdgcn_perm(wiv[u][e >> 1], wrv[u][e >> 1], (e & 1) ? 0x07060302u : 0x05040100u);
      ST_SC1(w, xoff + (OFF_WT - OFF_X1) + 2 * u * SLAB); }
    u32x4 rec = {pk_bf16(m, sd), epoch, w5r | (w5i << 16), __builtin_amdgcn_perm(wiv[0][0], wrv[0][0], 0x05040100u)};
    ST_SC1(rec, lane == 0 ? OFF_R + s * 16 : WS_USED);
    XPUT(12); XPUT(13); XPUT(14); XPUT(15);
  }
  }
  SCHED;
  STAMP(1);

  const int half = xg & 1, tm = (xg >> 1) * 4 + (rr & 3), tn = rr >> 2, tile = tm * 8 + tn;
  const int kt0 = half ? NA : 0, nkt = half ? 56 - NA : NA;
  const char* cB = Wd + (size_t)kt0 * 64 * (D_ * 2) + tn * 256;
#define G2_VOFF unsigned voffD; { int row = wid * 4 + fq; voffD = row * (D_ * 2) + ((fr ^ ((row & 7) << 1)) * 16); }
#define STD1(t, i) __builtin_amdgcn_global_load_lds((const unsigned*)(cB + (size_t)(t) * 64 * (D_ * 2) + (i) * 32 * (D_ * 2) + voffD), (LAS unsigned*)(lds + (((t) & 7) * 16384) + ldsw + (i) * 8192), 16, 0, 0)
#define G2_PRELOAD do { G2_VOFF; UNR for (int t_ = 0; t_ < 8; t_++) { STD1(t_, 0); STD1(t_, 1); } } while (0)

#define BLD(dst, ins, voff, mod) asm volatile(ins " %0, %1, %2, 0 offen " mod : "=v"(dst) : "v"(voff), "s"(rsW) : "memory")
#define P3_LOAD do { const int zo_ = OFF_Z + ((l31 >> 2) * 4 + hh) * SLAB + s * 64 + (((lane & 3) ^ sq) * 16), xo_ = l31 * 64 + hh * 16; \
    BLD(zz[0], "buffer_load_dwordx4", zo_, "sc1"); BLD(zz[1], "buffer_load_dwordx4", zo_ + 2 * SLAB, "sc1"); \
    GLD(zz[2], "global_load_dwordx4", xo_, X + tok * 2048, ""); GLD(zz[3], "global_load_dwordx4", xo_, X + tok * 2048, "offset:32"); } while (0)
#define P3_RUN(VC) do { u32x4 g1[4], tw3[4], frr[4]; \
    { const int ex = opq(L_EX + lane * 16) - L_EX, ea = opq(L_EX + ((lane & 51) | ((lane & 4) << 1) | ((lane & 8) >> 1)) * 16) - L_EX; \
      UNR for (int i = 0; i < 4; i++) { g1[i] = LDS4(ex + (L_G1 + i * 1024)); tw3[i] = LDS4(ea + (L_T1 + i * 1024)); frr[i] = LDS4(ex + (L_FR + i * 1024)); } } \
    const float sgn = hh ? 0.f : ((lane & 1) ? 1.f : -1.f); \
    asm volatile("s_waitcnt vmcnt(" #VC ")" : "+v"(zz[0]), "+v"(zz[1]), "+v"(zz[2]), "+v"(zz[3])::"memory"); \
    f32x16 re = MF32(zz[0], g1[0], ZERO16), im = MF32(zz[0], g1[2], ZERO16); \
    re = MF32(zz[1], g1[1], re); im = MF32(zz[1], g1[3], im); \
    const unsigned zq = __builtin_amdgcn_readlane((se & 2) ? ((se & 1) ? zz[0][3] : zz[0][2]) : ((se & 1) ? zz[0][1] : zz[0][0]), 0); \
    re[0] = __builtin_fmaf(sgn, bf_hi(zq), re[0]); \
    u32x4 up[4]; \
    UNR for (int i = 0; i < 16; i++) { float a_, c_; cmul(re[i], im[i], tw3[i >> 2][i & 3], a_, c_); up[i >> 2][i & 3] = pk_bf16(a_, c_); } \
    f32x16 y = ZERO16; \
    UNR for (int k = 0; k < 4; k++) y = MF32(up[k], frr[k], y); \
    u32x4 o[2]; \
    UNR for (int i = 0; i < 8; i++) { const unsigned xv = zz[2 + (i >> 2)][i & 3]; \
      o[i >> 2][i & 3] = pk_bf16(__builtin_fmaf(y[2 * i], 0x1p-20f, bf_lo(xv)), __builtin_fmaf(y[2 * i + 1], 0x1p-20f, bf_hi(xv))); } \
    UNR for (int e = 0; e < 4; e++) { auto sw = __builtin_amdgcn_permlane16_swap(o[0][e], o[1][e], false, false); o[0][e] = sw[0]; o[1][e] = sw[1]; } \
    const int to = OFF_T + tok * 2048 + fr * 64 + hh * 16 + (fq & 1) * 32; \
    ST_SC1(o[0], to); ST_SC1(o[1], to + 1024); } while (0)
  bool p3_done = false;

  if (isG) {
    LANES; G1_VOFF;
    const int aoff0 = (wr * 64 + fr) * 128 + ((fq ^ (fr & 7)) * 16), aoff1 = aoff0 ^ 64;
    int bo0, bo1;
    { int q = fr >> 2, p = lane & 3; int row = 8 * fq + q; int swz = ((row & 3) | ((row >> 1) & 4)) << 1;
      bo0 = row * 256 + (((4 * wc + (p >> 1)) ^ swz) * 16) + 8 * (p & 1) + 65536; bo1 = bo0 ^ 32; }
#define LDA(dst, bf, h) do { UNR for (int m = 0; m < 4; ++m) { dst[m][0] = LDS4(SA(bf, h) + aoff0 + m * 2048); dst[m][1] = LDS4(SA(bf, h) + aoff1 + m * 2048); } } while (0)
#define LDB(dst, bf, h) do { TRR(dst[0][0][0], bo0, SB(bf, h) - 65536); TRR(dst[0][0][1], bo0, SB(bf, h) - 65536 + 1024); \
    TRR(dst[1][0][0], bo1, SB(bf, h) - 65536); TRR(dst[1][0][1], bo1, SB(bf, h) - 65536 + 1024); \
    TRR(dst[0][1][0], bo0, SB(bf, h) - 65536 + 8192); TRR(dst[0][1][1], bo0, SB(bf, h) - 65536 + 9216); \
    TRR(dst[1][1][0], bo1, SB(bf, h) - 65536 + 8192); TRR(dst[1][1][1], bo1, SB(bf, h) - 65536 + 9216); } while (0)
#define WAIT_LB(B) asm volatile("s_waitcnt lgkmcnt(0)" : "+v"(B[0][0][0]), "+v"(B[0][0][1]), "+v"(B[0][1][0]), "+v"(B[0][1][1]), "+v"(B[1][0][0]), "+v"(B[1][0][1]), "+v"(B[1][1][0]), "+v"(B[1][1][1])::"memory")
#define MMA(ai, bj, Bt) do { __builtin_amdgcn_s_setprio(1); UNR for (int i = 0; i < 16; ++i) { const int m = i >> 2, n = ((i >> 1) ^ (i >> 2)) & 1, k = (i ^ (i >> 1)) & 1; \
    acc[ai][bj][m][n] = MF16(CAT(Bt[n][k]), At[m][k], acc[ai][bj][m][n]); SCHED; } __builtin_amdgcn_s_setprio(0); } while (0)
#define CSUM(on, Bt) do { if (on) { UNR for (int i = 0; i < 16; i++) cs[i & 1] = dot2(Bt[i & 1][(i >> 1) & 1][(i >> 2) & 1][i >> 3], 0x3f803f80u, cs[i & 1]); } SCHED; } while (0)
    f32x4 acc[2][2][4][2];
    UNR for (int a = 0; a < 2; a++) UNR for (int c = 0; c < 2; c++) UNR for (int m = 0; m < 4; m++) UNR for (int n = 0; n < 2; n++) acc[a][c][m][n] = (f32x4){0, 0, 0, 0};
    float cs[2] = {0.f, 0.f};
    const int tc = (rr & 3) * 2, tc0 = wr ? -1 : tc, tc1 = wr ? tc : -1;
    u32x4 At[4][2]; u32x2 B0[2][2][2], B1[2][2][2];
    BAR;
    if (wr == 1) BAR;
#define KTILE(bf, t, tp) do { \
    LDB(B0, bf, 0); SCHED; LDA(At, bf, 0); STA(SA(1 - bf, 1), 1, (t) + 1); \
    WAIT_L(8); BAR; WAIT_LB(B0); MMA(0, 0, B0); BAR; SCHED; \
    LDB(B1, bf, 1); STB(SB(bf, 0), cG, (t) + 2); CSUM(tp == tc0, B0); \
    BAR; WAIT_LB(B1); MMA(0, 1, B1); BAR; \
    LDA(At, bf, 1); STA(SA(bf, 0), 0, (t) + 2); \
    BAR; WAIT_L(0); MMA(1, 0, B0); BAR; SCHED; \
    STB(SB(bf, 1), cU, (t) + 2); CSUM(tp == tc1, B1); \
    WAIT_V(6); BAR; MMA(1, 1, B1); BAR; } while (0)
    for (int t = 0; t < 14; t += 2) {
      KTILE(0, t, t);
      STAMP(16 + t);
      if (t == E1) { TOUCH(Wd + (b * 512 + wid * 64 + lane_id()) * 64); SCHED; }
      if (t == 0 && tid == 0) FLAG_ST(OFF_F1 + b * 128);
      KTILE(1, t + 1, t);
      STAMP(17 + t);
      if (t == tc) {
        UNR for (int n = 0; n < 2; n++) { float v = cs[n];
          auto a = __builtin_amdgcn_permlane16_swap(__float_as_uint(v), __float_as_uint(v), false, false); v = __uint_as_float(a[0]) + __uint_as_float(a[1]);
          auto d = __builtin_amdgcn_permlane32_swap(__float_as_uint(v), __float_as_uint(v), false, false); cs[n] = __uint_as_float(d[0]) + __uint_as_float(d[1]); }
        const int l_ = lane_id();
        __builtin_amdgcn_raw_buffer_store_b64((u32x2){__float_as_uint((l_ & 16) ? cs[1] : cs[0]), epoch}, rsW, l_ < 32 ? OFF_CS + pn * 16384 + pm * 2048 + wid * 256 + l_ * 8 : WS_USED, 0, 16);
        SCHED; }
    }
    u32x2 rec, cq[4];
    const int ro = OFF_R + (pm * 256 + ((tid & 192) | ((tid & 1) << 5) | ((tid >> 1) & 31))) * 16, co = OFF_CS + pn * 16384 + wr * 8192 + (tid & 255) * 8;
#define EPI_LOAD do { BLD(rec, "buffer_load_dwordx2", ro, "sc1"); BLD(cq[0], "buffer_load_dwordx2", co, "sc1"); BLD(cq[1], "buffer_load_dwordx2", co, "offset:2048 sc1"); \
    BLD(cq[2], "buffer_load_dwordx2", co + 4096, "sc1"); BLD(cq[3], "buffer_load_dwordx2", co + 4096, "offset:2048 sc1"); } while (0)
#define EPI_WAIT asm volatile("s_waitcnt vmcnt(0)" : "+v"(rec), "+v"(cq[0]), "+v"(cq[1]), "+v"(cq[2]), "+v"(cq[3])::"memory")
    LDB(B0, 0, 0); LDA(At, 0, 0); STA(SA(1, 1), 1, 15); EPI_LOAD;
    BAR; WAIT_LB(B0); MMA(0, 0, B0); BAR;
    LDB(B1, 0, 1); BAR; WAIT_LB(B1); MMA(0, 1, B1); BAR;
    LDA(At, 0, 1); WAIT_V(9); BAR; WAIT_L(0); MMA(1, 0, B0); MMA(1, 1, B1); BAR;
    LDB(B0, 1, 0); LDA(At, 1, 0); WAIT_V(7); BAR; WAIT_LB(B0); MMA(0, 0, B0); BAR;
    LDB(B1, 1, 1); EPI_WAIT;
    for (unsigned sp = 0; sp < SPIN_CAP && !__all(rec[1] == epoch && cq[0][1] == epoch && cq[1][1] == epoch && cq[2][1] == epoch && cq[3][1] == epoch); ++sp) {
      __builtin_amdgcn_s_sleep(1); EPI_LOAD; EPI_WAIT; }
    unsigned f2;
    BLD(f2, "buffer_load_dword", wid == 0 ? OFF_F2 + rr * 128 + l31 * 4 : WS_USED, "sc1");
    { const float m = bf_lo(rec[0]), sd = bf_hi(rec[0]), r = __builtin_amdgcn_rcpf(sd);
      const float c0 = -2.302208198f;
      *(LAS f32x4*)(lds + L_EX + (tid & 255) * 16) = (f32x4){-m, c0 * r, c0 * 0.044715f * r * r * r, sd * sd};
      LDS1(L_EX + 4096 + tid * 4) = __float_as_uint((__uint_as_float(cq[0][0]) + __uint_as_float(cq[1][0])) + (__uint_as_float(cq[2][0]) + __uint_as_float(cq[3][0]))); }
    BAR; WAIT_LB(B1); MMA(0, 1, B1); BAR;
    LDA(At, 1, 1);
    BAR; WAIT_L(0); MMA(1, 0, B0); MMA(1, 1, B1);
    asm volatile("s_waitcnt vmcnt(0)" : "+v"(f2)::"memory");
    if (wid == 0) { LDS1(L_EX + 6144) = __all(f2 == epoch); WAIT_L(0); }
    BAR;
    if (wr == 0) BAR;
    STAMP(2);
    f32x4 rc[8], cg[2], cu[2];
    UNR for (int i = 0; i < 8; i++) rc[i] = *(LAS f32x4*)(lds + L_EX + ((i >> 2) * 128 + wr * 64 + (i & 3) * 16 + fr) * 16);
    UNR for (int n = 0; n < 2; n++) { const int o = L_EX + 4096 + (wc * 32 + n * 16 + fq * 4) * 4;
      cg[n] = *(LAS f32x4*)(lds + o) + *(LAS f32x4*)(lds + o + 1024); cu[n] = *(LAS f32x4*)(lds + o + 512) + *(LAS f32x4*)(lds + o + 1536); }
    p3_done = __builtin_amdgcn_readfirstlane(LDS1(L_EX + 6144)) != 0;
    u32x4 zz[4];
    P3_LOAD;
    SCHED; WAIT_L(0);
    G2_VOFF;
    const int hoff = OFF_H + ((pm * 16 + wr * 4) * 112 + pn * 4 + wc) * 1024 + lane * 16;
    UNR for (int i = 0; i < 8; i++) { float hv[8];
      UNR for (int j = 0; j < 8; j++) { const float gg = __builtin_fmaf(rc[i][0], cg[j >> 2][j & 3], acc[i >> 2][0][i & 3][j >> 2][j & 3]);
        const float uu = __builtin_fmaf(rc[i][0], cu[j >> 2][j & 3], acc[i >> 2][1][i & 3][j >> 2][j & 3]);
        const float ex = __builtin_amdgcn_exp2f(gg * __builtin_fmaf(gg * gg, rc[i][2], rc[i][1]));
        hv[j] = gg * uu * __builtin_amdgcn_rcpf(__builtin_fmaf(ex, rc[i][3], rc[i][3])); }
      ST_SC1(((u32x4){pk_bf16(hv[0], hv[1]), pk_bf16(hv[2], hv[3]), pk_bf16(hv[4], hv[5]), pk_bf16(hv[6], hv[7])}), hoff + ((i >> 2) * 8 + (i & 3)) * 112 * 1024);
      SCHED; STD1(i, 0); STD1(i, 1); SCHED; }
    XSTAMP(3);
    asm volatile("s_waitcnt vmcnt(24)" : "+v"(zz[0]), "+v"(zz[1]), "+v"(zz[2]), "+v"(zz[3])::"memory");
    if (p3_done) { P3_RUN(24); XSTAMP(4); WAIT_V(2); XPUT(4); } else WAIT_V(0);
    BAR;
    if (tid == 0) FLAG_ST(OFF_FH + (pm * 32 + pn) * 128);
    XPUT(3);
  } else {
    u32x4 FR[8], xc[8], xo[8];
#define FIX8(r, i) asm("" : "+v"(r[0]), "+v"(r[1]), "+v"(r[2]), "+v"(r[3]), "+v"(i[0]), "+v"(i[1]), "+v"(i[2]), "+v"(i[3]))
#define TWQ(dst, re, im, q, tw) do { float r_[4], i_[4]; \
      UNR for (int e = 0; e < 4; e++) { r_[e] = __builtin_fmaf(im[4 * (q) + e], TWH(tw[e], 1), 0.0f); i_[e] = __builtin_fmaf(im[4 * (q) + e], TWH(tw[e], 0), 0.0f); } SCHED; \
      UNR for (int e = 0; e < 4; e++) { r_[e] = __builtin_fmaf(re[4 * (q) + e], TWH(tw[e], 0), -r_[e]); i_[e] = __builtin_fmaf(re[4 * (q) + e], TWH(tw[e], 1), i_[e]); } \
      FIX8(r_, i_); UNR for (int e = 0; e < 4; e++) dst[e] = pk_bf16(r_[e], i_[e]); asm("" : "+v"(dst)); } while (0)
#define XBASE(c) (l31 * 64 + ((((wid & 1) + 2 * (c)) ^ ((l31 >> 1) & 3)) * 4 + ((wid >> 1) ^ (l31 >> 3))) * 4)
    {
    LANES;
    WAIT_ALL; BAR;
    if (tid == 0) FLAG_ST(OFF_F1 + b * 128);
    UNR for (int z = 0; z < 2; z++) {
      const float mult = (float)(z ? 2 * l31 + 1 : l31) * (1.0f / 64), b0 = (float)((8 * (wid & 3) + 4 * hh) * (2 - z) + 1 - z), ph = 0.25f * (float)(wid >> 2);
      u32x4 w;
      UNR for (int e = 0; e < 4; e++) { const float a = __builtin_fmaf(b0 + (float)(e * (2 - z)), mult, ph); w[e] = pk_bf16(cosr(a), cosr(a - 0.25f)); }
      LDS4((z ? L_O2 : L_O1) + wid * 1024 + lane * 16) = w; }
    TAB_TW(L_G1, 2, 1, (float)l31 * (1.0f / 2048));
    TAB_TW(L_TC, 1, 0, (float)(2 * l31 + 1) * (1.0f / 2048));
    UNR for (int i = 0; i < 4; i++) { FR[i] = LDS4(L_FR + i * 1024 + lane * 16);
      UNR for (int e = 0; e < 4; e++) FR[4 + i][e] = __builtin_amdgcn_alignbit(FR[i][e], FR[i][e], 16) ^ 0x8000u; }
    if (wid < 4) POLLS(OFF_F1 + tid * 128, 4);
    WAIT_L(0); BAR;
    if (g == 0 && tid == 0) FLAG_ST(OFF_E);
    STAMP(2);
    u32x4 sx[16], sw[16]; u32x2 rp[4], rn[4];
    const int so = g * SLAB + wid * 16384 + lane * 16;
    UNR for (int e = 0; e < 4; e++) { const int j = l31 + 32 * (8 * wid + 4 * hh + e), o = g ? WS_USED : OFF_R + 8;
      rp[e] = __builtin_amdgcn_raw_buffer_load_b64(rsW, o + j * 16, 0, 16); rn[e] = __builtin_amdgcn_raw_buffer_load_b64(rsW, o + ((2048 - j) & 2047) * 16, 0, 16); }
    UNR for (int i = 0; i < 16; i++) sx[i] = LD_SC1(OFF_X1 + so + i * 1024);
    UNR for (int i = 0; i < 16; i++) sw[i] = LD_SC1(OFF_WT + so + i * 1024);
    SCHED;
    if (g == 0) {
      u32x4 f1, f2;
      UNR for (int e = 0; e < 4; e++) {
        const float ar = bf_lo(rp[e][1]) + bf_lo(rn[e][1]), ai = bf_hi(rp[e][1]) - bf_hi(rn[e][1]), br = bf_lo(rp[e][0]) + bf_lo(rn[e][0]), bi = bf_hi(rp[e][0]) - bf_hi(rn[e][0]);
        f1[e] = pk_bf16(0.125f * (ar + br), 0.125f * (ai + bi)); f2[e] = pk_bf16(0.125f * (ar - br), 0.125f * (ai - bi)); }
      *(u32x4*)(ws + OFF_FF + tid * 16) = f1; *(u32x4*)(ws + OFF_FF + 8192 + tid * 16) = f2;
    }
    UNR for (int i = 0; i < 16; i++) LDS4(wid * 16384 + i * 1024 + lane * 16) = sx[i];
    WAIT_L(0); BAR;
    { const int x0 = XBASE(0) + hh * 16384;
      UNR for (int j = 0; j < 4; j++) { const int b0 = opq(x0 + (j >> 1) * 2048 + (j & 1) * 65536), b1 = opq(b0 ^ 32);
        UNR for (int i = 0; i < 8; i++) { xc[2 * j + (i >> 2)][i & 3] = LDS1(b0 + (i >> 2) * 32768 + (i & 3) * 4096); xo[2 * j + (i >> 2)][i & 3] = LDS1(b1 + (i >> 2) * 32768 + (i & 3) * 4096); } } }
    WAIT_L(0); BAR;
    STAMP(3);
    int wsb[8];
    UNR for (int e = 0; e < 8; e++) wsb[e] = opq(wid * 16384 + (lane >> 2) * 64 + (((e & 3) ^ ((lane >> 3) & 3)) * 4 + ((lane & 3) ^ ((lane >> 3) & 3) ^ hh ^ ((e >> 2) * 2))) * 4);
#define WPUT(i0) do { UNR for (int i = (i0); i < (i0) + 4; i++) UNR for (int e = 0; e < 4; e++) LDS1(wsb[(i & 1) * 4 + e] + i * 1024) = sw[i][e]; SCHED; } while (0)
    const int ex = opq(L_EX + lane * 16) - L_EX;
#define STAGE_A(x, W0) do { f32x16 r0 = ZERO16, i0 = ZERO16, r1 = ZERO16, i1 = ZERO16; u32x4 tw[4]; \
      UNR for (int k = 0; k < 4; k++) { tw[k] = LDS4(ex + (L_T1 + k * 1024)); r0 = MF32(x[k], FR[k], r0); i0 = MF32(x[k], FR[4 + k], i0); } SCHED; \
      WPUT(W0); \
      UNR for (int q = 0; q < 4; q++) { r1 = MF32(x[4 + q], FR[q], r1); SCHED; TWQ(x[q], r0, i0, q, tw[q]); i1 = MF32(x[4 + q], FR[4 + q], i1); tw[q] = LDS4(ex + (L_G1 + q * 1024)); SCHED; } \
      WPUT(W0 + 4); \
      UNR for (int q = 0; q < 4; q++) { TWQ(x[4 + q], r1, i1, q, tw[q]); SCHED; } } while (0)
    STAGE_A(xc, 0); STAGE_A(xo, 8);
    WAIT_V(0); WAIT_L(0); BAR;
    STAMP(6);
    }
#pragma unroll 1
    for (int c = 0; c < 2; c++) {
      LANES;
      const int ex = opq(L_EX + lane * 16) - L_EX, xb0 = XBASE(c);
      const int wb[2] = {opq(xb0 + hh * 8192), opq(xb0 + hh * 8192 + 65536)};
      f32x16 er = ZERO16, ei = ZERO16, xr, xi, cr = ZERO16, ci = ZERO16;
      u32x4 bq[4], w0, w1, fp[8], tw[4];
#define C1_LOAD do { UNR for (int k = 0; k < 2; k++) { fp[k] = LDS4(ex + (L_O2 + k * 1024)); fp[4 + k] = LDS4(ex + (L_O2 + 4096 + k * 1024)); } tw[0] = LDS4(ex + L_T1); } while (0)
      { u32x4 fo[8];
        UNR for (int k = 0; k < 8; k++) fo[k] = LDS4(ex + (L_O1 + k * 1024));
        UNR for (int k = 0; k < 4; k++) { er = MF32(FR[k], xc[k], er); ei = MF32(FR[4 + k], xc[k], ei); }
        SCHED;
        UNR for (int e = 0; e < 4; e++) { w0[e] = LDS1(wb[0] + e * 2048); w1[e] = LDS1(wb[1] + e * 2048); }
        SCHED;
        xr = MF32(fo[0], xc[4], er); xi = MF32(fo[4], xc[4], ei);
        UNR for (int k = 1; k < 4; k++) { xr = MF32(fo[k], xc[4 + k], xr); xi = MF32(fo[4 + k], xc[4 + k], xi); }
        SCHED; }
      if (g == 0 && wid == 0 && c == 0) {
        u32x4 ff[16];
        UNR for (int q = 0; q < 16; q++) ff[q] = *(const u32x4*)(ws + OFF_FF + ((q & 3) * 4 + (q >> 2)) * 1024 + lane * 16);
        UNR for (int i = 0; i < 16; i++) { er[i] = __builtin_fmaf(2.f, er[i], -xr[i]); ei[i] = __builtin_fmaf(2.f, ei[i], -xi[i]);
          LDS1(wb[0] + ((i & 3) + 8 * (i >> 2)) * 2048) = pk_bf16(xr[i], xi[i]); LDS1(wb[1] + ((i & 3) + 8 * (i >> 2)) * 2048) = pk_bf16(er[i], ei[i]); }
        const int lm = (32 - l31) & 31, m0 = (32 - l31) * 64 + (((lm >> 1) & 3) * 4 + (lm >> 3)) * 4 + (4 - 4 * hh) * 2048, mb[2] = {opq(m0 + 65536), opq(m0)};
        SCHED;
        UNR for (int q = 0; q < 4; q++) { u32x4 aq;
          UNR for (int e = 0; e < 4; e++) { float yr[2], yi[2];
            UNR for (int nu = 0; nu < 2; nu++) { const unsigned mm = LDS1(e + q + nu == 0 ? (lane == 0 ? 0 : mb[0] + 27 * 2048) : mb[nu] + (27 - e - 8 * q) * 2048);
              const float vr = nu ? er[4 * q + e] : xr[4 * q + e], vi = nu ? ei[4 * q + e] : xi[4 * q + e], mr = bf_lo(mm), mi = bf_hi(mm);
              const unsigned f1 = ff[4 * q + nu][e], f2 = ff[4 * q + 2 + nu][e];
              yr[nu] = vr * bf_lo(f1) - vi * bf_hi(f1) + mr * bf_lo(f2) + mi * bf_hi(f2);
              yi[nu] = -vr * bf_hi(f1) - vi * bf_lo(f1) - mr * bf_hi(f2) + mi * bf_lo(f2); }
            aq[e] = pk_bf16(yr[0] + yr[1], yi[0] + yi[1]); bq[q][e] = pk_bf16(yr[0] - yr[1], yi[0] - yi[1]); }
          cr = MF32(aq, FR[q], cr); ci = MF32(aq, FR[4 + q], ci); SCHED; }
        C1_LOAD;
      } else {
        UNR for (int q = 0; q < 4; q++) { float ar[4], ai[4], br[4], bi[4], vr[4], vi[4], wl[4], wh[4]; u32x4 aq; const u32x4 u0 = w0, u1 = w1;
          if (q < 3) UNR for (int e = 0; e < 4; e++) { w0[e] = LDS1(wb[0] + (e + 8 * q + 8) * 2048); w1[e] = LDS1(wb[1] + (e + 8 * q + 8) * 2048); }
          else C1_LOAD;
          UNR for (int e = 0; e < 4; e++) { wl[e] = bf_lo(u0[e]); wh[e] = bf_hi(u0[e]); } SCHED;
          UNR for (int e = 0; e < 4; e++) { br[e] = xi[4 * q + e] * wh[e]; bi[e] = xi[4 * q + e] * wl[e]; } FIX8(br, bi); SCHED;
          UNR for (int e = 0; e < 4; e++) { vr[e] = __builtin_fmaf(2.f, er[4 * q + e], -xr[4 * q + e]); vi[e] = __builtin_fmaf(2.f, ei[4 * q + e], -xi[4 * q + e]); } SCHED;
          UNR for (int e = 0; e < 4; e++) { br[e] = __builtin_fmaf(xr[4 * q + e], wl[e], -br[e]); bi[e] = __builtin_fmaf(-xr[4 * q + e], wh[e], -bi[e]); } SCHED;
          UNR for (int e = 0; e < 4; e++) { wl[e] = bf_lo(u1[e]); wh[e] = bf_hi(u1[e]); } SCHED;
          UNR for (int e = 0; e < 4; e++) { ar[e] = __builtin_fmaf(-vi[e], wh[e], br[e]); ai[e] = __builtin_fmaf(-vi[e], wl[e], bi[e]); } SCHED;
          UNR for (int e = 0; e < 4; e++) { ar[e] = __builtin_fmaf(vr[e], wl[e], ar[e]); ai[e] = __builtin_fmaf(-vr[e], wh[e], ai[e]); } SCHED;
          UNR for (int e = 0; e < 4; e++) { br[e] = __builtin_fmaf(2.f, br[e], -ar[e]); bi[e] = __builtin_fmaf(2.f, bi[e], -ai[e]); }
          FIX8(ar, ai); FIX8(br, bi); SCHED;
          UNR for (int e = 0; e < 4; e++) aq[e] = pk_bf16(ar[e], ai[e]); SCHED;
          UNR for (int e = 0; e < 2; e++) bq[q][e] = pk_bf16(br[e], bi[e]); SCHED;
          cr = MF32(aq, FR[q], cr); SCHED;
          UNR for (int e = 2; e < 4; e++) bq[q][e] = pk_bf16(br[e], bi[e]);
          asm("" : "+v"(bq[q])); SCHED;
          ci = MF32(aq, FR[4 + q], ci); SCHED; }
      }
      const int zb[2] = {opq(xb0 + hh * 16384), opq(xb0 + hh * 16384 + 65536)};
      f32x16 dr = ZERO16, di = ZERO16;
      er = ZERO16; ei = ZERO16; xr = ZERO16; xi = ZERO16;
      { UNR for (int k = 2; k < 4; k++) { fp[k] = LDS4(ex + (L_O2 + k * 1024)); fp[4 + k] = LDS4(ex + (L_O2 + 4096 + k * 1024)); }
        UNR for (int k = 1; k < 4; k++) tw[k] = LDS4(ex + (L_T1 + k * 1024));
        SCHED;
        UNR for (int k = 0; k < 2; k++) { er = MF32(bq[k], fp[k], er); ei = MF32(bq[k], fp[4 + k], ei); }
        SCHED;
        UNR for (int q = 0; q < 4; q++) { u32x4 up; TWQ(up, cr, ci, q, tw[q]); tw[q] = LDS4(ex + (L_TC + q * 1024)); SCHED;
          if (q & 1) ei = MF32(bq[2 + (q >> 1)], fp[6 + (q >> 1)], ei); else er = MF32(bq[2 + (q >> 1)], fp[2 + (q >> 1)], er);
          SCHED; dr = MF32(FR[q], up, dr); di = MF32(FR[4 + q], up, di); SCHED; }
        UNR for (int q = 0; q < 4; q++) { u32x4 up; TWQ(up, er, ei, q, tw[q]); SCHED;
          if (q) UNR for (int e = 0; e < 2; e++) LDS1(zb[(q - 1) >> 1] + ((q - 1) & 1) * 32768 + e * 4096) = pk_bf16(dr[4 * q - 4 + e], di[4 * q - 4 + e]);
          SCHED; xr = MF32(FR[q], up, xr); SCHED;
          if (q) UNR for (int e = 2; e < 4; e++) LDS1(zb[(q - 1) >> 1] + ((q - 1) & 1) * 32768 + e * 4096) = pk_bf16(dr[4 * q - 4 + e], di[4 * q - 4 + e]);
          SCHED; xi = MF32(FR[4 + q], up, xi); SCHED; }
        UNR for (int e = 0; e < 4; e++) LDS1(zb[1] + 32768 + e * 4096) = pk_bf16(dr[12 + e], di[12 + e]);
        UNR for (int k = 0; k < 8; k++) xc[k] = xo[k];
        SCHED;
        UNR for (int i = 0; i < 16; i++) LDS1(zb[i >> 3] + ((i >> 2) & 1) * 32768 + (i & 3) * 4096 + 2048) = pk_bf16(xr[i], xi[i]); }
    }
    STAMP(4);
    __builtin_amdgcn_s_setprio(0);
    {
    LANES;
    WAIT_L(0); BAR;
    XSTAMP(33);
    UNR for (int i = 0; i < 16; i++) ST_SC1(LDS4(wid * 16384 + i * 1024 + lane * 16), OFF_Z + g * SLAB + wid * 16384 + i * 1024 + lane * 16);
    XSTAMP(34);
    TAB_G1;
    WAIT_V(0); XSTAMP(35); WAIT_L(0); BAR;
    XPUT(33); XPUT(34); XPUT(35);
    if (tid < 32) FLAG_ST(OFF_F2 + tid * 128 + g * 4);
    STAMP(5);
    G2_PRELOAD;
    SCHED;
    }
  }
  STAMP(7);
  if (!p3_done) {
    LANES;
    if (wid == 0) POLL1(OFF_F2 + rr * 128 + l31 * 4);
    BAR;
    u32x4 zz[4];
    P3_LOAD; P3_RUN(0);
  }
  STAMP(8);

  {
    LANES;
    const int kg = wid >> 2, rg = wid & 3;
    if (wid == 0) { const int np = nkt >> 1; POLL1(OFF_FH + ((tm >> 1) * 32 + (kt0 >> 1) + (lane < np ? lane : 0)) * 128); }
    G2_VOFF;
    const int va0 = OFF_H + ((tm * 8 + rg * 2 + kg) * 112 + kt0 * 2 + kg) * 1024 + lane * 16, va1 = OFF_H + ((tm * 8 + rg * 2 + (kg ^ 1)) * 112 + kt0 * 2 + kg) * 1024 + lane * 16;
    int bo[8];
    { int q = fr >> 2, p = lane & 3; int row = 32 * kg + 4 * fq + q; int base = row * 256 + (((p >> 1) ^ ((row & 7) << 1)) * 16) + 8 * (p & 1);
      UNR for (int n = 0; n < 8; n++) bo[n] = base ^ (n * 32); }
    const int row = tm * 128 + (rg * 2 + kg) * 16 + fr, colo = tn * 128 + (fq & 1) * 16 + (fq >> 1) * 8;
    const int po = OFF_P + tile * 32768 + wid * 4096 + lane * 16;
    const int eo = half ? OFF_T + row * (D_ * 2) + colo * 2 : po, es = half ? 64 : 1024;
    f32x4 acc[2][8];
    UNR for (int a = 0; a < 2; a++) UNR for (int n = 0; n < 8; n++) acc[a][n] = (f32x4){0, 0, 0, 0};
    u32x4 Ar[4][2], ev[4]; u32x2 B0[8][2], B1[8][2];
    if (!isG) BAR;
    UNR for (int n = 0; n < 8; n++) { TRR(B0[n][0], bo[n], 0); TRR(B0[n][1], bo[n], 4096); }
    BAR;
    STAMP(9);
#define LDH1(sl, t, i) asm volatile("buffer_load_dwordx4 %0, %1, %2, %3 offen sc1" : "=v"(Ar[sl][i]) : "v"((i) ? va1 : va0), "s"(rsW), "s"((t) * 2048) : "memory")
#define LDE(u) asm volatile("buffer_load_dwordx4 %0, %1, %2, %3 offen sc1" : "=v"(ev[u]) : "v"(eo), "s"(rsW), "s"((u) * es) : "memory")
#define WAIT_LD(B) asm volatile("s_waitcnt lgkmcnt(0)" : "+v"(B[0][0]), "+v"(B[0][1]), "+v"(B[1][0]), "+v"(B[1][1]), "+v"(B[2][0]), "+v"(B[2][1]), "+v"(B[3][0]), "+v"(B[3][1]), \
   "+v"(B[4][0]), "+v"(B[4][1]), "+v"(B[5][0]), "+v"(B[5][1]), "+v"(B[6][0]), "+v"(B[6][1]), "+v"(B[7][0]), "+v"(B[7][1])::"memory")
#define WAIT_VA(n, sl) asm volatile("s_waitcnt vmcnt(" #n ")" : "+v"(Ar[sl][0]), "+v"(Ar[sl][1])::"memory")
#define FLIP do { UNR for (int n = 0; n < 8; n++) bo[n] ^= 65536; } while (0)
#define ITER(t, Bc, Bn, sl, so, VC, ISB, ISA, RD, DOBAR) do { WAIT_VA(VC, sl); WAIT_LD(Bc); if (DOBAR) BAR; SCHED; \
    UNR for (int n = 0; n < 8; n++) { \
      if (RD) { TRR(Bn[n][0], bo[n], so); TRR(Bn[n][1], bo[n], so + 4096); } \
      acc[n & 1][n] = MF16(CAT(Bc[n]), Ar[sl][n & 1], acc[n & 1][n]); SCHED; \
      acc[(n & 1) ^ 1][n] = MF16(CAT(Bc[n]), Ar[sl][(n & 1) ^ 1], acc[(n & 1) ^ 1][n]); SCHED; \
      if (ISB && n == 0) STD1((t) + 7, 0); if (ISB && n == 2) STD1((t) + 7, 1); \
      if (ISA == 1 && n == 4) LDH1(((sl) + 3) & 3, (t) + 3, 0); if (ISA == 1 && n == 6) LDH1(((sl) + 3) & 3, (t) + 3, 1); \
      if (ISA == 2 && n < 4) LDE(n); SCHED; } } while (0)
    LDH1(0, 0, 0); LDH1(0, 0, 1); LDH1(1, 1, 0); LDH1(1, 1, 1); LDH1(2, 2, 0); LDH1(2, 2, 1);
    ITER(0, B0, B1, 0, 16384, 4, 0, 1, 1, 1);
    if (tid == 0) FLAG_ST(OFF_F3 + b * 128);
    ITER(1, B1, B0, 1, 32768, 4, 1, 1, 1, 0);
    ITER(2, B0, B1, 2, 49152, 6, 1, 1, 1, 1); FLIP; ITER(3, B1, B0, 3, 0, 8, 1, 1, 1, 0);
    int t;
    for (t = 4; t < nkt - 8; t += 4) {
      ITER(t, B0, B1, 0, 16384, 8, 1, 1, 1, 1); ITER(t + 1, B1, B0, 1, 32768, 8, 1, 1, 1, 0);
      ITER(t + 2, B0, B1, 2, 49152, 8, 1, 1, 1, 1); FLIP; ITER(t + 3, B1, B0, 3, 0, 8, 1, 1, 1, 0); }
    ITER(t, B0, B1, 0, 16384, 8, 1, 1, 1, 1); ITER(t + 1, B1, B0, 1, 32768, 8, 0, 1, 1, 0);
    ITER(t + 2, B0, B1, 2, 49152, 6, 0, 1, 1, 1); FLIP; ITER(t + 3, B1, B0, 3, 0, 4, 0, 1, 1, 0);
    ITER(t + 4, B0, B1, 0, 16384, 4, 0, 1, 1, 1); ITER(t + 5, B1, B0, 1, 32768, 4, 0, 0, 1, 0);
    if (wid == 0) POLL1(half ? OFF_F3 + (16 * tm + fr) * 128 : OFF_FP + tile * 128);
    ITER(t + 6, B0, B1, 2, 49152, 2, 0, 2, 1, 1); ITER(t + 7, B1, B0, 3, 0, 4, 0, 0, 0, 1);
    STAMP(10);
    UNR for (int n = 0; n < 8; n++) *(LAS f32x4*)(lds + wid * 8192 + n * 1024 + lane * 16) = acc[1][n];
    WAIT_L(0); BAR;
    UNR for (int n = 0; n < 8; n++) acc[0][n] += *(LAS f32x4*)(lds + (wid ^ 4) * 8192 + n * 1024 + lane * 16);
    UNR for (int u = 0; u < 4; u++) UNR for (int j = 0; j < 4; j++) {
      auto sw = __builtin_amdgcn_permlane16_swap(__float_as_uint(acc[0][2 * u][j]), __float_as_uint(acc[0][2 * u + 1][j]), false, false);
      acc[0][2 * u][j] = __uint_as_float(sw[0]); acc[0][2 * u + 1][j] = __uint_as_float(sw[1]); }
    asm volatile("s_waitcnt vmcnt(0)" : "+v"(ev[0]), "+v"(ev[1]), "+v"(ev[2]), "+v"(ev[3])::"memory");
    UNR for (int u = 0; u < 4; u++) { const f32x4 lo = acc[0][2 * u], hi = acc[0][2 * u + 1];
      const u32x4 w = {pk_bf16(lo[0] + bf_lo(ev[u][0]), lo[1] + bf_hi(ev[u][0])), pk_bf16(lo[2] + bf_lo(ev[u][1]), lo[3] + bf_hi(ev[u][1])),
                       pk_bf16(hi[0] + bf_lo(ev[u][2]), hi[1] + bf_hi(ev[u][2])), pk_bf16(hi[2] + bf_lo(ev[u][3]), hi[3] + bf_hi(ev[u][3]))};
      if (half) ST_SC1(w, po + u * 1024); else *(u32x4*)(O + row * (D_ * 2) + (colo + 32 * u) * 2) = w; }
    if (half) { WAIT_V(0); BAR; if (tid == 0) FLAG_ST(OFF_FP + tile * 128); }
    STAMP(11);
    if (wid == 0 && (ENCW)) for (int sp = 0; sp < 100000 && __builtin_amdgcn_s_memrealtime() < enc_ + 8000; sp++) __builtin_amdgcn_s_sleep(1);
  }
}

extern "C" void kernel_launch(void* const* d_in, const int* in_sizes, int n_in, void* d_out, int out_size, void* d_ws, size_t ws_size, hipStream_t stream) {
  (void)in_sizes; (void)n_in; (void)out_size; (void)ws_size;
  (void)hipFuncSetAttribute((const void*)fused_k, hipFuncAttributeMaxDynamicSharedMemorySize, LDS_BYTES);
  fused_k<<<256, 512, LDS_BYTES, stream>>>((const char*)d_in[0], (const char*)d_in[1], (const char*)d_in[2], (const char*)d_in[4],
                                           (const char*)d_in[5], (const char*)d_in[6], (char*)d_out, (char*)d_ws);
}
```

```cpp
#include <hip/hip_runtime.h>
#include <stdint.h>
#include <type_traits>
#define LAS __attribute__((address_space(3)))
#define GAS __attribute__((address_space(1)))
typedef short bf16x8 __attribute__((ext_vector_type(8)));
typedef float f32x4 __attribute__((ext_vector_type(4)));
typedef float f32x16 __attribute__((ext_vector_type(16)));
typedef unsigned u32x4 __attribute__((ext_vector_type(4)));
typedef unsigned u32x2 __attribute__((ext_vector_type(2)));
typedef unsigned u32x16 __attribute__((ext_vector_type(16)));
typedef u32x4 u32x4u __attribute__((aligned(2)));
typedef __bf16 bf16x2v __attribute__((ext_vector_type(2)));
typedef _Float16 h16x2 __attribute__((ext_vector_type(2)));
typedef float f32x2 __attribute__((ext_vector_type(2)));

constexpr int S_ = 2048, D_ = 1024, F_ = 3584;
constexpr int OFF_E = 0, OFF_F2 = 4096, OFF_FW = 8192, OFF_FP = 16384, OFF_R = 32768, OFF_FF = 65536, OFF_CS = 131072, OFF_F1 = 655360, OFF_F3 = OFF_F1 + 32768, OFF_FH = OFF_F3 + 32768, OFF_FB = OFF_FH + 64;
constexpr int SLAB = 135424;
constexpr int OFF_X1 = 1 << 20, OFF_WT = 6 << 20, OFF_Z = 11 << 20, OFF_T = 16 << 20, OFF_P = 20 << 20, OFF_H = 24 << 20, WS_USED = 40 << 20;
constexpr int L_EX = 131072, L_T1 = L_EX + 8192, L_G1 = L_EX + 12288, L_FR = L_EX + 16384, L_SC = L_EX + 24576, LDS_BYTES = 163840;
constexpr int L_RS = L_EX + 28672, L_S2 = 20480;
constexpr int L_O1 = L_EX, L_O2 = L_SC, L_TC = L_EX + 20480;
constexpr int NA = 32;
constexpr unsigned SPIN_CAP = 1u << 18;

__device__ __forceinline__ unsigned pk_bf16(float lo, float hi) { f32x2 v = {lo, hi}; return __builtin_bit_cast(unsigned, __builtin_convertvector(v, bf16x2v)); }
__device__ __forceinline__ unsigned pk_f16(float lo, float hi) { h16x2 v = {(_Float16)lo, (_Float16)hi}; return __builtin_bit_cast(unsigned, v); }
__device__ __forceinline__ float bf_lo(unsigned u) { return __uint_as_float(u << 16); }
__device__ __forceinline__ float bf_hi(unsigned u) { return __uint_as_float(u & 0xffff0000u); }
__device__ __forceinline__ float rbf(float f) { return bf_lo(pk_bf16(f, f)); }
__device__ __forceinline__ float cosr(float rev) { return __builtin_amdgcn_cosf(rev); }
__device__ __forceinline__ float dot2(unsigned a, unsigned b, float c) { return __builtin_amdgcn_fdot2_f32_bf16(__builtin_bit_cast(bf16x2v, a), __builtin_bit_cast(bf16x2v, b), c, false); }
__device__ __forceinline__ void cmul(float re, float im, unsigned t, float& r2, float& i2) {
  h16x2 tw = __builtin_bit_cast(h16x2, t);
  float a = __builtin_fmaf(im, (float)tw[1], 0.0f), b = __builtin_fmaf(im, (float)tw[0], 0.0f);
  r2 = __builtin_fmaf(re, (float)tw[0], -a); i2 = __builtin_fmaf(re, (float)tw[1], b);
  asm("" : "+v"(r2)); asm("" : "+v"(i2));
}
__device__ __forceinline__ float TWH(unsigned t, int h) { h16x2 tw = __builtin_bit_cast(h16x2, t); return (float)tw[h]; }
template <int CTRL> __device__ __forceinline__ float dppf(float v) { return __builtin_bit_cast(float, __builtin_amdgcn_update_dpp(0, __builtin_bit_cast(int, v), CTRL, 0xf, 0xf, true)); }
__device__ __forceinline__ float par_sum(float v) {
  v += dppf<0x4E>(v); v += dppf<0x124>(v); v += dppf<0x128>(v);
  auto a = __builtin_amdgcn_permlane16_swap(__float_as_uint(v), __float_as_uint(v), false, false); v = __uint_as_float(a[0]) + __uint_as_float(a[1]);
  auto b = __builtin_amdgcn_permlane32_swap(__float_as_uint(v), __float_as_uint(v), false, false); return __uint_as_float(b[0]) + __uint_as_float(b[1]);
}
__device__ __forceinline__ float rdl(float v, int l) { return __builtin_bit_cast(float, __builtin_amdgcn_readlane(__builtin_bit_cast(int, v), l)); }

__device__ __forceinline__ int opq(int v) { asm volatile("" : "+v"(v)); return v; }
__device__ __forceinline__ int ops(int v) { asm volatile("" : "+s"(v)); return v; }
__device__ __forceinline__ int lane_id() { int v; asm volatile("v_mbcnt_lo_u32_b32 %0, -1, 0\n\tv_mbcnt_hi_u32_b32 %0, -1, %0" : "=v"(v)); __builtin_assume(v >= 0 && v < 64); return v; }
#define LANES const int lane = lane_id(), tid = wid * 64 + lane, l31 = lane & 31, hh = lane >> 5, fr = lane & 15, fq = lane >> 4; (void)tid; (void)l31; (void)hh; (void)fr; (void)fq
#define BC8(x) __builtin_bit_cast(bf16x8, x)
#define MF32(a, b, c) __builtin_amdgcn_mfma_f32_32x32x16_bf16(BC8(a), BC8(b), c, 0, 0, 0)
#define MF16(a, b, c) __builtin_amdgcn_mfma_f32_16x16x32_bf16(BC8(a), BC8(b), c, 0, 0, 0)
#define MF16P(c, a, b, d, e) asm volatile("v_mfma_f32_16x16x32_bf16 %0, %1, %2, %0\n\tv_mfma_f32_16x16x32_bf16 %0, %3, %4, %0" : "+v"(c) : "v"(a), "v"(b), "v"(d), "v"(e))
#define WAIT_V(n) asm volatile("s_waitcnt vmcnt(" #n ")" ::: "memory")
#define WAIT_L(n) asm volatile("s_waitcnt lgkmcnt(" #n ")" ::: "memory")
#define WAIT_ALL do { __builtin_amdgcn_s_waitcnt(0x0070); asm volatile("" ::: "memory"); } while (0)
#define BAR __builtin_amdgcn_s_barrier()
#define BAR_IF(v, c, vc) asm volatile("s_cmp_lg_u32 %0, %1\n\ts_cbranch_scc1 1f\n\ts_waitcnt vmcnt(%2)\n\ts_barrier\n1:" :: "s"(v), "n"(c), "n"(vc) : "scc", "memory")
#define SCHED __builtin_amdgcn_sched_barrier(0)
#define UNR _Pragma("unroll")
#define LDS4(off) (*(LAS u32x4*)(lds + (off)))
#define LDS1(off) (*(LAS unsigned*)(lds + (off)))
#define ST_SC1(v, off) __builtin_amdgcn_raw_buffer_store_b128(v, rsW, off, 0, 16)
#define ST_SO(v, off, so) asm volatile("buffer_store_dwordx4 %0, %1, %2, %3 offen sc1\n\ts_nop 1" :: "v"(v), "v"(off), "s"(rsW), "s"(so) : "memory")
#define LD_SC1(off) __builtin_amdgcn_raw_buffer_load_b128(rsW, off, 0, 16)
#define FLAG_ST(off) __hip_atomic_store((GAS unsigned*)(ws + (off)), epoch, __ATOMIC_RELAXED, __HIP_MEMORY_SCOPE_AGENT)
#define FLAG_LD(off) __hip_atomic_load((GAS unsigned*)(ws + (off)), __ATOMIC_RELAXED, __HIP_MEMORY_SCOPE_AGENT)
#define POLLS(off, slp) do { const int po_ = (off); _Pragma("unroll 1") for (unsigned sp_ = 0; sp_ < SPIN_CAP && FLAG_LD(po_) != epoch; ++sp_) __builtin_amdgcn_s_sleep(slp); \
    __builtin_amdgcn_fence(__ATOMIC_ACQUIRE, "wavefront"); } while (0)
#define POLL1(off) POLLS(off, 1)
#define TRR(dst, base, off) asm volatile("ds_read_b64_tr_b16 %0, %1 offset:%2" : "=v"(dst) : "v"(base), "i"(off) : "memory")
#define CAT(x) __builtin_shufflevector(x[0], x[1], 0, 1, 2, 3)
#define KRV 8
#define ZPOS 1
#define FINE 1
#define PL 0
#define TNM 1
#define SLP 2
#define OAUX 16
#define ZL 0
#define GL -1
#define PB 3
#define FPOS 1
#define WN 2
#define WT 8
#define R1 0
#define R1T 4
#define R2M 0
#define E1F 1
static const f32x16 ZERO16 = {0, 0, 0, 0, 0, 0, 0, 0, 0, 0, 0, 0, 0, 0, 0, 0};

__global__ __launch_bounds__(512, 2) void fused_k(const char*, const char*, const char*, const char*, const char*, const char*, char*, char*) {
  u32x16 ka;
  asm volatile("s_load_dwordx16 %0, %1, 0x0" : "=s"(ka) : "s"(__builtin_amdgcn_kernarg_segment_ptr()));
  extern __shared__ __attribute__((aligned(16))) char smem_[];
  LAS char* lds = (LAS char*)smem_;
  const int b = blockIdx.x, xg = b & 7, rr = b >> 3;
  const bool isG = rr < 28;
  const int g = xg * 4 + (rr & 3);
  const int wid = __builtin_amdgcn_readfirstlane(threadIdx.x >> 6);
#define SA(bf, h) (((bf) * 2 + (h)) * 16384)
#define SB(bf, h) ((4 + (bf) * 2 + (h)) * 16384)
  {
    LANES;
    const int hi = wid >> 2, e2 = ((l31 >> 1) & 1) + 2 * ((l31 >> 3) & 1), k2 = 4 * (2 * (l31 >> 4) + ((l31 >> 2) & 1)) + e2;
    const float mult = (float)(hi ? k2 : l31) * (1.0f / 32), ph = 0.25f * (float)((l31 & hi) + (((wid >> 1) & 1) ^ -hi) + hi);
    const float t0 = __builtin_fmaf((float)(16 * (wid & 1) + 4 * hh), mult, ph);
    float v[8];
    UNR for (int e = 0; e < 8; e++) v[e] = cosr(__builtin_fmaf((float)((e & 3) + 8 * (e >> 2)), mult, t0));
    LDS4((isG ? SB(1, 1) : L_SC) + wid * 1024 + lane * 16) = (u32x4){pk_bf16(v[0], v[1]), pk_bf16(v[2], v[3]), pk_bf16(v[4], v[5]), pk_bf16(v[6], v[7])};
  }
  SCHED;
  asm volatile("s_waitcnt lgkmcnt(0)" : "+s"(ka)::"memory");
#define ARG(i) ((char*)(GAS char*)(((unsigned long long)ka[2 * (i) + 1] << 32) | ka[2 * (i)]))
  const char *const X = ARG(0), *const Wr = ARG(1), *const Wi = ARG(2), *const Wg = ARG(3), *const Wu = ARG(4), *const Wd = ARG(5);
  char *const O = ARG(6), *const ws = ARG(7);
  const auto rsW = __builtin_amdgcn_make_buffer_rsrc(ws, 0, WS_USED, 0x00020000);
  const int pm = (xg & 1) * 4 + (rr & 3), pn = (xg >> 1) * 7 + (rr >> 2);
  const int wr = wid >> 2, wc = wid & 3, kr = (xg & 1) * KRV;
  const auto cA = __builtin_amdgcn_make_buffer_rsrc((void*)(X + pm * 256 * (D_ * 2)), 0, 256 * D_ * 2, 0x00020000);
  const auto cG = __builtin_amdgcn_make_buffer_rsrc((void*)(Wg + pn * 256), 0, D_ * F_ * 2, 0x00020000);
  const auto cU = __builtin_amdgcn_make_buffer_rsrc((void*)(Wu + pn * 256), 0, D_ * F_ * 2, 0x00020000);
  const unsigned ldsw = wid * 1024;
#define G1_VOFF { int row = wid * 8 + (lane >> 3); voffA = row * (D_ * 2) + (((lane & 7) ^ (lane >> 3)) * 16); } \
  { int row = wid * 4 + fq; int swz = ((row & 3) | ((row >> 1) & 4)) << 1; voffB = row * (F_ * 2) + ((fr ^ swz) * 16); }
#define NTB 2
  const int rho = (rr + (rr >> 2)) & 3, kr2 = kr ^ (rho & R1);
#define KX(t) ((t) ^ ((t) < R1T ? kr : kr2))
#define DMA(rs, ldsoff, voff, soff) __builtin_amdgcn_raw_ptr_buffer_load_lds(rs, (LAS void*)(lds + (ldsoff)), 16, voff, soff, 0, 0)
#define DMB(rs, ldsoff, voff, soff) __builtin_amdgcn_raw_ptr_buffer_load_lds(rs, (LAS void*)(lds + (ldsoff)), 16, voff, soff, 0, NTB)
#define STA(bufoff, h, t) do { UNR for (int i_ = 0; i_ < 2; ++i_) DMA(cA, (bufoff) + ldsw + i_ * 8192, voffA, ((h) * 128 + i_ * 64) * (D_ * 2) + KX(t) * 128); } while (0)
#define STB(bufoff, base, t) do { UNR for (int i_ = 0; i_ < 2; ++i_) DMB(base, (bufoff) + ldsw + i_ * 8192, voffB, (KX(t) * 64 + i_ * 32) * (F_ * 2)); } while (0)
  const int s = b * 8 + wid, sz = (s >> 1) & 15;
  const int tok = (s & ~63) | ((s & 31) << 1) | ((s >> 5) & 1);
#define TOUCH(p, l) __builtin_amdgcn_global_load_lds((const unsigned*)(p), (LAS unsigned*)(lds + (l) + wid * 256), 4, 0, 0)
#define GLD(dst, ins, voff, sbase, mod) asm volatile(ins " %0, %1, %2 " mod : "=v"(dst) : "v"(voff), "s"(sbase) : "memory")
#define TAB_TW(dst, ca, cb, ml) do { const float m2 = (ml), t0 = (float)((ca) * (2 * (wid & 1) + 8 * (wid >> 1) + 4 * hh) + (cb)) * m2, t1 = t0 + (float)(ca) * m2; \
    *(LAS u32x2*)(lds + (dst) + (wid >> 1) * 1024 + lane * 16 + (wid & 1) * 8) = (u32x2){pk_f16(cosr(t0), cosr(t0 + 0.25f)), pk_f16(cosr(t1), cosr(t1 + 0.25f))}; } while (0)
#define TAB_G1 do { const int a = (l31 & 19) | ((l31 & 4) << 1) | ((l31 & 8) >> 1); \
    const float mult = (float)(wid < 4 ? a : l31) * (1.0f / 32), sc = wid < 4 ? 0x1p-20f : 1.f; \
    const float u0 = __builtin_fmaf((float)(8 * (wid & (wid < 4 ? 1 : 3)) + 4 * hh), mult, (wid >> 1) == 1 ? 0.25f : 0.f); \
    u32x4 w; \
    UNR for (int e = 0; e < 4; e++) { const float u = __builtin_fmaf((float)e, mult, u0); w[e] = pk_bf16(cosr(u) * sc, cosr(u - 0.25f) * sc); } \
    LDS4(L_G1 + wid * 1024 + lane * 16) = w; } while (0)
  unsigned epoch;
  u32x4 wt;
#define WSH(r) ((2 * (r)) & 12)
#define WLD(mat, ptr) do { const auto rw = __builtin_amdgcn_make_buffer_rsrc((void*)(ptr), 0, S_ * 1026, 0x00020000); \
    UNR for (int r = 0; r < 8; r++) DMA(rw, wid * 16896 + ((mat) * 8 + r) * 1056 + 16 - WSH(r), lane * 16, (g * 8 + wid) * 8208 + r * 1024); } while (0)
  f32x4 acc[2][2][4][2];
  unsigned voffA, voffB;
  int aoff0, aoff1, bo0, bo1, ro, co;
  f32x4 cs; u32x4 sel[2];
  auto prologue = [&](auto gc) __attribute__((always_inline)) {
  constexpr bool isG = decltype(gc)::value;
  constexpr int L_ROW = isG ? SB(1, 1) : L_SC;
  LANES;
  unsigned e0, w5r, w5i; u32x4 xa[2];
  { const int xo_ = l31 * 64 + hh * 16;
    GLD(xa[0], "global_load_dwordx4", xo_, X + tok * 2048, ""); GLD(xa[1], "global_load_dwordx4", xo_, X + tok * 2048, "offset:32"); }
  SCHED;
  TAB_TW(L_T1, 1, 0, (float)l31 * (1.0f / 1024));
  G1_VOFF;
  WAIT_L(0); BAR;
  {
    u32x4 tb[12];
    { const int tr_ = opq(L_ROW + lane * 16), te_ = opq(L_T1 + lane * 16);
      UNR for (int i = 0; i < 12; i++) tb[i] = LDS4(i < 8 ? tr_ + i * 1024 : te_ + (i - 8) * 1024); }
    SCHED;
#define E0LD do { GLD(e0, "global_load_dword", 0, ws + OFF_E, "sc1"); \
    GLD(w5r, "global_load_ushort", (lane & 1) * 1024, Wr + s * 1026, ""); GLD(w5i, "global_load_ushort", (lane & 1) * 1024, Wi + s * 1026, ""); } while (0)
    if (isG) { STB(SB(0, 0), cG, 0); STA(SA(0, 0), 0, 0); } else { E0LD; const char* tp = ((lane & 8) ? Wi : Wr) + (g * 8 + wid) * 8208 + ((lane & 7) + 1) * 1024;
      asm volatile("global_load_dwordx4 %0, %1, off" : "=v"(wt) : "v"(tp) : "memory"); SCHED; if (WN > 0) WLD(0, Wr); }
    SCHED;
    if (!isG || GL < 0) TAB_G1;
    u32x4 idm[2];
    UNR for (int j = 0; j < 2; j++) { const int d = l31 - 16 * j - 8 * hh;
      UNR for (int p = 0; p < 4; p++) idm[j][p] = (d >> 1) == p ? (0x3f80u << (16 * (d & 1))) : 0u; }
    if (isG) UNR for (int a = 0; a < 32; a++) { f32x4& c = acc[a >> 4][(a >> 3) & 1][(a >> 1) & 3][a & 1];
      if (ZL && a >= 8) asm volatile("" : "=v"(c)); else { c = (f32x4){0, 0, 0, 0}; asm volatile("" : "+v"(c)); } }
    if (isG) { aoff0 = opq((wr * 64 + fr) * 128 + ((fq ^ (fr & 7)) * 16)); aoff1 = opq(aoff0 ^ 64);
      const int q = fr >> 2, p = lane & 3, row = 8 * fq + q, swz = ((row & 3) | ((row >> 1) & 4)) << 1;
      bo0 = opq(row * 256 + (((2 * wc + (p >> 1)) ^ swz) * 16) + 8 * (p & 1) + 65536); bo1 = opq(bo0 ^ 128);
      ro = opq(OFF_R + (pm * 256 + wc * 64 + (((lane & 1) << 5) | (lane >> 1))) * 16); co = opq(OFF_CS + pn * 16384 + wr * 8192 + wc * 512 + lane * 8); }
    if (isG) { cs = (f32x4){0.f, 0.f, 0.f, 0.f}; asm volatile("" : "+v"(cs));
      UNR for (int n = 0; n < 2; n++) { const unsigned o = (fr >> 3) == n ? 0x3f803f80u : 0u; sel[n] = (u32x4){o, o, o, o}; asm volatile("" : "+v"(sel[n])); } }
    asm volatile("" : "+v"(idm[0]), "+v"(idm[1]));
    SCHED;
    if (isG) { E0LD; STB(SB(0, 1), cU, 0); STA(SA(0, 1), 1, 0); } else if (WN > 1) WLD(1, Wi);
    SCHED;
    asm volatile("s_waitcnt vmcnt(%2)" : "+v"(xa[0]), "+v"(xa[1]) : "n"(isG ? 11 : 4 + 8 * WN) : "memory");
#define EARLY(i) do { if (isG && PB == (i)) { SCHED; WAIT_L(0); BAR_IF(wr, 1, 7); SCHED; } } while (0)
    EARLY(0);
    f32x16 xt = MF32(xa[0], idm[0], ZERO16); xt = MF32(xa[1], idm[1], xt);
    float s0 = 0.f, s1 = 0.f;
    UNR for (int j = 0; j < 2; j++) UNR for (int p = 0; p < 4; p++) { s0 = dot2(xa[j][p], 0x3f803f80u, s0); s1 = dot2(xa[j][p], 0xbf803f80u, s1); }
    const float sv = par_sum(((lane & 1) ? s1 : s0) + dppf<0xB1>((lane & 1) ? s0 : s1));
    const float mu = rdl(sv, 0) * (1.0f / D_), alt = rdl(sv, 1), m = rbf(mu);
    float q = 0.f;
    UNR for (int i = 0; i < 16; i++) { xt[i] -= m; q = __builtin_fmaf(xt[i], xt[i], q); }
    u32x4 df[2];
    UNR for (int i = 0; i < 8; i++) df[i >> 2][i & 3] = pk_bf16(xt[2 * i], xt[2 * i + 1]);
    f32x16 tr = MF32(df[0], tb[0], ZERO16), ti = MF32(df[0], tb[2], ZERO16);
    tr = MF32(df[1], tb[1], tr); ti = MF32(df[1], tb[3], ti);
    q = par_sum(q + dppf<0xB1>(q));
    const float var = fmaxf(q * (1.0f / D_) - (mu - m) * (mu - m), 0.f);
    const float sd = rbf(__builtin_amdgcn_sqrtf(rbf(rbf(var) + 1e-5f))), rs = __builtin_amdgcn_rcpf(sd);
    SCHED; EARLY(1);
    u32x4 br[2], bi[2];
    UNR for (int i = 0; i < 8; i++) { float r0, i0, r1, i1;
      cmul(tr[2 * i], ti[2 * i], tb[8 + (i >> 1)][(2 * i) & 3], r0, i0); cmul(tr[2 * i + 1], ti[2 * i + 1], tb[8 + (i >> 1)][(2 * i + 1) & 3], r1, i1);
      br[i >> 2][i & 3] = pk_bf16(r0, r1); bi[i >> 2][i & 3] = pk_bf16(i0, i1); if (i == 3) EARLY(2); }
    f32x16 xo = MF32(tb[4], br[0], ZERO16); xo = MF32(tb[6], bi[0], xo); xo = MF32(tb[5], br[1], xo); xo = MF32(tb[7], bi[1], xo);
    xo[1] = lane == 0 ? alt : xo[1];
    EARLY(3);
    const int so = L_EX + hh * 4096 + (l31 >> 4) * 512 + wid * 64 + ((fr ^ sz) * 4);
    const int go = OFF_X1 + (8 * hh + (l31 >> 4)) * SLAB + s * 64 + ((fr ^ sz) * 4);
    UNR for (int j = 0; j < 8; j++) { float v0 = xo[2 * j] * rs; asm("" : "+v"(v0)); const unsigned v = pk_bf16(v0, xo[2 * j + 1] * rs);
      if (isG) LDS1(so + (j >> 2) * L_S2 + (j & 3) * 1024) = v; else __builtin_amdgcn_raw_buffer_store_b32(v, rsW, go, (16 * (j >> 2) + 2 * (j & 3)) * SLAB, 16); }
    SCHED;
    if (isG) { STB(SB(1, 0), cG, 1); STA(SA(1, 0), 0, 1); }
    asm volatile("s_waitcnt vmcnt(%3)" : "+v"(e0), "+v"(w5r), "+v"(w5i) : "n"(isG ? 8 : 8 + 8 * WN) : "memory");
    epoch = __builtin_amdgcn_readfirstlane(e0) * 0x9E3779B1u + 0x7F4A7C15u;
    const unsigned w0 = w5r | (w5i << 16), w5 = __builtin_amdgcn_readlane(w0, 1);
    if (isG) { if (lane == 0) LDS4(L_RS + wid * 16) = (u32x4){pk_bf16(m, sd), epoch, w5, w0}; }
    else { asm volatile("" : "+v"(wt)); ST_SC1(((u32x4){pk_bf16(m, sd), epoch, w5, w0}), lane == 0 ? OFF_R + s * 16 : WS_USED); SCHED; if (WN < 1) WLD(0, Wr); if (WN < 2) WLD(1, Wi); }
    if (isG) { SCHED; BAR_IF(wr, 0, 63); STB(SB(1, 1), cU, 1); } else { WAIT_L(0); BAR; }
  }
  SCHED;
  };
  const int half = xg & 1, tm = (xg >> 1) * 4 + (rr & 3), tn = TNM ? (rr >> 2) ^ ((xg >> 1) == 2 && (rr >> 3) == 1 ? 3 : 2) : rr >> 2, tile = tm * 8 + tn;
  const int kt0 = half ? NA : 0, nkt = half ? 56 - NA : NA;
  const auto cB = __builtin_amdgcn_make_buffer_rsrc((void*)(Wd + kt0 * 64 * (D_ * 2) + tn * 256), 0, F_ * D_ * 2, 0x00020000);
#define G2_VOFF { int row = wid * 4 + fq; voffD = row * (D_ * 2) + ((fr ^ ((row & 7) << 1)) * 16); }
  const int np = nkt >> 1;
#define KT(t) (2 * ((t) ^ (rho & R2M)) - ((t) < np ? 0 : nkt - 1))
#define STD1(t, i) DMA(cB, ((t) & 7) * 16384 + ldsw + (i) * 8192, voffD, (KT(t) * 64 + (i) * 32) * (D_ * 2))
#define G2_PRELOAD do { unsigned voffD; G2_VOFF; UNR for (int t_ = 0; t_ < 8; t_++) { STD1(t_, 0); STD1(t_, 1); } } while (0)
#define BLD(dst, ins, voff, mod) asm volatile(ins " %0, %1, %2, 0 offen " mod : "=v"(dst) : "v"(voff), "s"(rsW) : "memory")
#define P3_ADDR const int zo_ = opq(OFF_Z + (8 * hh + (l31 >> 4)) * SLAB + s * 64 + ((fr ^ sz) * 4)), xo_ = opq(l31 * 64 + hh * 16)
#define P3_LOADZ(z_, c) asm volatile("buffer_load_dword %0, %1, %2, %3 offen sc1" : c(zd[z_]) : "v"(zo_), "s"(rsW), "s"((16 * ((z_) >> 2) + 2 * ((z_) & 3)) * SLAB) : "memory")
#define P3_LOADX do { GLD(zx[0], "global_load_dwordx4", xo_, X + tok * 2048, ""); GLD(zx[1], "global_load_dwordx4", xo_, X + tok * 2048, "offset:32"); } while (0)
#define P3_WAIT(VC) asm volatile("s_waitcnt vmcnt(%10)" : "+v"(zd[0]), "+v"(zd[1]), "+v"(zd[2]), "+v"(zd[3]), "+v"(zd[4]), "+v"(zd[5]), "+v"(zd[6]), "+v"(zd[7]), "+v"(zx[0]), "+v"(zx[1]) : "n"(VC) : "memory")
#define HFLAG(o) __builtin_amdgcn_raw_buffer_store_b32(epoch, rsW, tid < 16 ? (o) + ((pm * 16 + tid) * 2 + (pn >> 4)) * 128 + (pn & 15) * 4 : WS_USED, 0, 16)
  bool p3_early = false;
  unsigned zd[8]; u32x4 zx[2];
  if (__builtin_expect(isG, 1)) {
    prologue(std::true_type{});
    LANES;
#define LDA(dst, bf, h) do { UNR for (int m = 0; m < 4; ++m) { dst[m][0] = LDS4(SA(bf, h) + aoff0 + m * 2048); dst[m][1] = LDS4(SA(bf, h) + aoff1 + m * 2048); } } while (0)
#define LDB(dst, bf, h) do { TRR(dst[0][0][0], bo0, SB(bf, h) - 65536); TRR(dst[0][0][1], bo0, SB(bf, h) - 65536 + 1024); \
    TRR(dst[1][0][0], bo1, SB(bf, h) - 65536); TRR(dst[1][0][1], bo1, SB(bf, h) - 65536 + 1024); \
    TRR(dst[0][1][0], bo0, SB(bf, h) - 65536 + 8192); TRR(dst[0][1][1], bo0, SB(bf, h) - 65536 + 9216); \
    TRR(dst[1][1][0], bo1, SB(bf, h) - 65536 + 8192); TRR(dst[1][1][1], bo1, SB(bf, h) - 65536 + 9216); } while (0)
#define WAIT_LB(B) asm volatile("s_waitcnt lgkmcnt(0)" : "+v"(B[0][0][0]), "+v"(B[0][0][1]), "+v"(B[0][1][0]), "+v"(B[0][1][1]), "+v"(B[1][0][0]), "+v"(B[1][0][1]), "+v"(B[1][1][0]), "+v"(B[1][1][1])::"memory")
#define MMA(ai, bj, Bt) do { __builtin_amdgcn_s_setprio(1); UNR for (int i = 0; i < 8; ++i) { const int m = i >> 1, n = (i ^ (i >> 1)) & 1, k = i & 1; \
    MF16P(acc[ai][bj][m][n], CAT(Bt[n][k]), At[m][k], CAT(Bt[n][k ^ 1]), At[m][k ^ 1]); SCHED; } __builtin_amdgcn_s_setprio(0); } while (0)
#define CSUM(on, Bt) do { if (on) { MF16P(cs, CAT(Bt[0][0]), sel[0], CAT(Bt[0][1]), sel[0]); MF16P(cs, CAT(Bt[1][0]), sel[1], CAT(Bt[1][1]), sel[1]); } } while (0)
    const int tc = ((rr & 3) * 2) ^ ((rr & 3) * 2 < R1T ? 0 : rho & R1 & 2), tc0 = wr ? -1 : tc, tc1 = wr ? tc : -1;
    u32x4 At[4][2]; u32x2 B0[2][2][2], B1[2][2][2];
    u32x4 sa, sb; int so;
    u32x2 rec, cq[4];
    asm volatile("" : "=v"(rec), "=v"(cq[0]), "=v"(cq[1]), "=v"(cq[2]), "=v"(cq[3]));
#define EPI_LD(dst, voff, o, t, c) do { int s_; asm volatile("s_cmp_lg_u32 %4, %5\n\ts_cbranch_scc1 1f\n\ts_movk_i32 %1, %6\n\tbuffer_load_dwordx2 %0, %2, %3, %1 offen sc1\n1:" \
    : "+v"(dst), "=&s"(s_) : "v"(voff), "s"(rsW), "s"(t), "n"(c), "n"(o) : "scc", "memory"); } while (0)
    unsigned voffD; int hoff, rco, cso, rwo, cwo;
#define WAITF(n, tp) do { WAIT_V(n); if (tp) WAIT_V(10); } while (0)
#define ZERO(ai, bj) do { if (ZL) UNR for (int a = 0; a < 8; a++) acc[ai][bj][a >> 1][a & 1] = (f32x4){0, 0, 0, 0}; } while (0)
#define KTILE(bf, t, tp) do { \
    LDB(B0, bf, 0); SCHED; LDA(At, bf, 0); STA(SA(1 - bf, 1), 1, (t) + 1); \
    if (bf) { WAIT_V(11); if (tp) WAIT_V(10); else if (wc >= 2) ST_SO(sb, so, 16 * SLAB); } else WAIT_V(10); \
    WAIT_L(8); if (bf) BAR; else asm volatile("s_cmp_eq_u32 %0, 0\n\ts_cbranch_scc1 1f\n\ts_barrier\n1:" :: "s"(tp + wr) : "scc", "memory"); \
    WAIT_LB(B0); MMA(0, 0, B0); CSUM(tp == tc0, B0); BAR; SCHED; \
    if (bf == 0 && tp == 0) { const int t_ = wid * 64 + lane_id(); sa = LDS4(L_EX + t_ * 16); sb = LDS4(L_EX + L_S2 + t_ * 16); so = OFF_X1 + (t_ >> 5) * SLAB + b * 512 + (t_ & 31) * 16; ZERO(0, 1); SCHED; } \
    LDB(B1, bf, 1); STB(SB(bf, 0), cG, (t) + 2); SCHED; \
    if (bf) WAITF(12, tp); else { WAIT_V(10); if (tp == 0 && wc < 2) ST_SC1(sa, so); } \
    BAR; WAIT_LB(B1); MMA(0, 1, B1); CSUM(tp == tc1, B1); BAR; \
    if (FPOS == 0 && bf == 0 && tp + wid * 16 == 2 && lane_id() == 0) FLAG_ST(OFF_F1 + b * 128); \
    LDA(At, bf, 1); STA(SA(bf, 0), 0, (t) + 2); \
    if (bf == 0 && tp == 0) { if (wc >= 2) ST_SC1(sa, so); ZERO(1, 0); } \
    BAR; WAIT_L(0); MMA(1, 0, B0); BAR; SCHED; \
    if (bf && tp == tc) { const int l_ = lane_id(); \
      ST_SC1(((u32x4){__float_as_uint((l_ & 1) ? cs[2] : cs[0]), epoch, __float_as_uint((l_ & 1) ? cs[3] : cs[1]), epoch}), (l_ & 6) ? WS_USED : OFF_CS + ops(pn) * 16384 + ops(pm) * 2048 + ops(wid) * 256 + (l_ & 9) * 16 + (l_ >> 4) * 32); } \
    STB(SB(bf, 1), cU, (t) + 2); SCHED; \
    if (GL >= 0 && bf == 0 && tp == GL) { LANES; TAB_G1; SCHED; } \
    WAIT_V(11); if (tp) WAIT_V(10); else if (bf) { if (FPOS) WAIT_V(6); } else { ZERO(1, 1); if (wc < 2) ST_SO(sb, so, 16 * SLAB); \
      if (wid == 2) { const int l_ = lane_id(); ST_SC1(LDS4(L_RS + (l_ & 7) * 16), l_ < 8 ? OFF_R + ops(b) * 128 + l_ * 16 : ops(WS_USED)); } } \
    if (bf) EPI_LD(cq[0], co, 0, tp, 8); else { EPI_LD(rec, ro, 0, tp, 8); EPI_LD(cq[3], co, 6144, tp, 12); } EPI_LD(cq[1 + bf], co, 2048 + 2048 * bf, tp, 10); \
    if (bf && tp == 12) { const int w_ = ops(wid), pm_ = ops(pm), pn_ = ops(pn); { const int wid = w_, wr = wid >> 2, wc = wid & 3, pm = pm_, pn = pn_; LANES; G2_VOFF; voffD = opq(voffD); \
      hoff = opq(OFF_H + ((pm * 16 + wr * 4 + hh) * 112 + pn * 4 + (wc >> 1)) * 1024 + ((wc & 1) * 32 + l31) * 16); \
      rco = opq(L_EX + (wr * 64 + fr) * 16); cso = opq(L_EX + 4096 + (wc * 32 + fq * 4) * 4); rwo = opq(L_EX + (tid & 255) * 16); cwo = opq(L_EX + 4096 + tid * 4); } } \
    BAR; MMA(1, 1, B1); BAR; \
    if (FPOS == 1 && bf == 1 && (tp | wid) == 0) __builtin_amdgcn_raw_buffer_store_b32(ops(epoch), rsW, lane_id() == 0 ? OFF_F1 + ops(b) * 128 : ops(WS_USED), 0, 16); } while (0)
#pragma unroll 1
    for (int t = 0; t < 14; t += 2) {
      asm volatile("" : "+s"(t));
      KTILE(0, t, t);
      if (!E1F && t == 6) { TOUCH(Wd + (b * 512 + wid * 64 + lane_id()) * 64, L_SC); SCHED; }
      KTILE(1, t + 1, t);
    }
#define STD2(t) do { STD1(t, 0); STD1(t, 1); } while (0)
#define EPI_WAIT(n) asm volatile("s_waitcnt vmcnt(" #n ")" : "+v"(rec), "+v"(cq[0]), "+v"(cq[1]), "+v"(cq[2]), "+v"(cq[3])::"memory")
    LDB(B0, 0, 0); LDA(At, 0, 0); STA(SA(1, 1), 1, 15);
    EPI_WAIT(10); BAR; WAIT_LB(B0); MMA(0, 0, B0); BAR;
    _Pragma("unroll 1") for (unsigned sp = 0; sp < SPIN_CAP && !__all((opq((rec[1] ^ epoch) | (cq[0][1] ^ epoch)) | opq((cq[3][1] ^ epoch) | opq((cq[1][1] ^ epoch) | (cq[2][1] ^ epoch)))) == 0); ++sp) {
      __builtin_amdgcn_s_sleep(1); EPI_LD(rec, ro, 0, 0, 0); UNR for (int j = 0; j < 4; j++) EPI_LD(cq[j], co, j * 2048, 0, 0); EPI_WAIT(0); }
    LDB(B1, 0, 1); SCHED;
    WAIT_V(8); BAR; WAIT_LB(B1); MMA(0, 1, B1); BAR;
    LDA(At, 0, 1); SCHED; STD2(4); STD2(0); WAIT_V(10); BAR; WAIT_L(0); MMA(1, 0, B0); BAR;
    LDB(B0, 1, 0); STD2(5); SCHED;
    { const float m = bf_lo(rec[0]), sd = bf_hi(rec[0]), r = __builtin_amdgcn_rcpf(sd);
      float c1 = -2.302208198f * r, c3 = r * r, c2 = __uint_as_float(cq[0][0]) + __uint_as_float(cq[1][0]);
      asm("" : "+v"(c1)); asm("" : "+v"(c2)); asm("" : "+v"(c3));
      c3 = c3 * 0.044715f * c1; asm("" : "+v"(c3));
      *(LAS f32x4*)(lds + rwo) = (f32x4){-m, c1, c3, sd * sd};
      LDS1(cwo) = __float_as_uint(c2 + (__uint_as_float(cq[2][0]) + __uint_as_float(cq[3][0]))); }
    SCHED; WAIT_V(8); BAR; MMA(1, 1, B1); BAR;
    LDB(B1, 1, 1); SCHED; LDA(At, 1, 0);
#define MMN(ai, n, lo) do { SCHED; __builtin_amdgcn_s_setprio(3); UNR for (int i_ = 0; i_ < 8; ++i_) { const int m = i_ >> 1, k = i_ & 1; \
    if ((i_ ^ (i_ >> 1)) & 1) MF16P(acc[ai][1][m][n], CAT(B1[n][k]), At[m][k], CAT(B1[n][k ^ 1]), At[m][k ^ 1]); \
    else MF16P(acc[ai][0][m][n], CAT(B0[n][k]), At[m][k], CAT(B0[n][k ^ 1]), At[m][k ^ 1]); SCHED; } __builtin_amdgcn_s_setprio(lo); } while (0)
    WAIT_V(6); WAIT_L(8); BAR; WAIT_LB(B0); WAIT_LB(B1); MMN(0, 0, 2); BAR_IF(wr, 0, 63);
    f32x4 rc[8], cg[2], cu[2];
#define RCLD(i) do { rc[2 * (i)] = *(LAS f32x4*)(lds + rco + ((i) >> 1) * 2048 + ((i) & 1) * 512); rc[2 * (i) + 1] = *(LAS f32x4*)(lds + rco + ((i) >> 1) * 2048 + ((i) & 1) * 512 + 256); } while (0)
#define CSLD(n) do { cg[n] = *(LAS f32x4*)(lds + cso + (n) * 64) + *(LAS f32x4*)(lds + cso + (n) * 64 + 1024); cu[n] = *(LAS f32x4*)(lds + cso + (n) * 64 + 512) + *(LAS f32x4*)(lds + cso + (n) * 64 + 1536); } while (0)
    RCLD(0); CSLD(0); SCHED; LDA(At, 1, 1); SCHED;
    unsigned f2;
    P3_ADDR;
#define H2(v, j) ((f32x2){v[2 * ((j) & 1)], v[2 * ((j) & 1) + 1]})
#define R2(j, e) ((f32x2){rc[2 * i + ((j) >> 1)][e], rc[2 * i + ((j) >> 1)][e]})
#define AC(bj, j, n) acc[i >> 1][bj][2 * (i & 1) + ((j) >> 1)][n]
#define MI(bj, m, k) do { if (mi) { SCHED; asm volatile("v_mfma_f32_16x16x32_bf16 %0, %1, %2, %0" : "+v"(acc[i < 2][bj][m][i > 0]) : "v"(bj ? CAT(B1[i > 0][k]) : CAT(B0[i > 0][k])), "v"(At[m][k])); SCHED; } } while (0)
#define UU1(n) uu[j] = __builtin_elementwise_fma(R2(j, 0), H2(cu[n], j), H2(AC(1, j, n), j))
#define EX1(e) do { if (mi == 2) { ex[j][e] = __builtin_fmaf(ex[j][e], rc[2 * i + (j >> 1)][3], rc[2 * i + (j >> 1)][3]); SCHED; } } while (0)
#define GEGLU(n) UNR for (int i = 0; i < 4; i++) { f32x2 gg[4], uu[4], ex[4]; u32x4 hv; const int mi = !n && i < 3 ? FINE : 0; \
      if (n && i == 0) BLD(f2, "buffer_load_dword", OFF_F2 + rr * 128 + l31 * 4, "sc1"); \
      UNR for (int j = 0; j < 4; j++) gg[j] = __builtin_elementwise_fma(R2(j, 0), H2(cg[n], j), H2(AC(0, j, n), j)); \
      UNR for (int j = 0; j < 4; j++) ex[j] = gg[j] * gg[j]; \
      if (mi) UNR for (int j = 0; j < 4; j++) UU1(n); \
      UNR for (int j = 0; j < 4; j++) ex[j] = __builtin_elementwise_fma(ex[j], R2(j, 2), R2(j, 1)); \
      if (mi) UNR for (int j = 0; j < 4; j++) uu[j] = uu[j] * gg[j]; \
      UNR for (int j = 0; j < 4; j++) ex[j] = ex[j] * gg[j]; \
      SCHED; \
      UNR for (int j = 0; j < 4; j++) { MI(0, j, 0); ex[j][0] = __builtin_amdgcn_exp2f(ex[j][0]); MI(1, j, 0); ex[j][1] = __builtin_amdgcn_exp2f(ex[j][1]); if (!mi) UU1(n); \
        if (!n && i < 3 && j < 2) STD1(i == 0 ? 1 : 5 + i, j); \
        if (n && i == 0 && j == 0) P3_LOADX; if (n && i == 3) STD1(2 + (j >> 1), j & 1); SCHED; } \
      if (mi < 2) UNR for (int j = 0; j < 4; j++) ex[j] = __builtin_elementwise_fma(ex[j], R2(j, 3), R2(j, 3)); \
      if (!n && i < 3) RCLD(i + 1); if (!n && i == 3) CSLD(1); \
      SCHED; \
      UNR for (int j = 0; j < 4; j++) { EX1(0); EX1(1); MI(0, j, 1); ex[j][0] = __builtin_amdgcn_rcpf(ex[j][0]); MI(1, j, 1); ex[j][1] = __builtin_amdgcn_rcpf(ex[j][1]); if (!mi) uu[j] = uu[j] * gg[j]; SCHED; } \
      if (!n && i == 1) { LDA(At, 1, 0); SCHED; } \
      UNR for (int j = 0; j < 4; j++) uu[j] = uu[j] * ex[j]; \
      UNR for (int j = 0; j < 4; j++) hv[j] = pk_bf16(uu[j][0], uu[j][1]); \
      UNR for (int j = 0; j < 2; j++) { auto sw = __builtin_amdgcn_permlane32_swap(hv[j], hv[2 + j], false, false); hv[j] = sw[0]; hv[2 + j] = sw[1]; } \
      ST_SO(hv, hoff, (((i >> 1) * 8 + (i & 1) * 2) * 112 + 2 * (n)) * 1024); \
      if (n && i == ZPOS) { asm volatile("s_waitcnt vmcnt(%1)" : "+v"(f2) : "n"(3 + ZPOS) : "memory"); LDS1(cwo + 2048) = f2; WAIT_L(0); BAR; \
        f2 = LDS1((cwo | 1024) + 2048); HFLAG(OFF_FH); UNR for (int j = 0; j < 8; j++) P3_LOADZ(j, "=v"); } \
      SCHED; }
    GEGLU(0);
    __builtin_amdgcn_s_setprio(1);
    GEGLU(1);
    __builtin_amdgcn_s_setprio(0);
    p3_early = __all(f2 == epoch);
    if (!PL) P3_WAIT(7 - ZPOS);
  } else {
    u32x4 FR[8], xc[8], xo[8];
#define FIX8(r, i) asm("" : "+v"(r[0]), "+v"(r[1]), "+v"(r[2]), "+v"(r[3]), "+v"(i[0]), "+v"(i[1]), "+v"(i[2]), "+v"(i[3]))
#define TWQ(dst, re, im, q, tw) do { float r_[4], i_[4]; \
      UNR for (int e = 0; e < 4; e++) { r_[e] = __builtin_fmaf(im[4 * (q) + e], TWH(tw[e], 1), 0.0f); i_[e] = __builtin_fmaf(im[4 * (q) + e], TWH(tw[e], 0), 0.0f); } SCHED; \
      UNR for (int e = 0; e < 4; e++) { r_[e] = __builtin_fmaf(re[4 * (q) + e], TWH(tw[e], 0), -r_[e]); i_[e] = __builtin_fmaf(re[4 * (q) + e], TWH(tw[e], 1), i_[e]); } \
      FIX8(r_, i_); UNR for (int e = 0; e < 4; e++) dst[e] = pk_bf16(r_[e], i_[e]); asm("" : "+v"(dst)); } while (0)
#define XBASE(c) (l31 * 64 + ((wid + 8 * (c)) ^ (l31 >> 1)) * 4)
    {
    prologue(std::false_type{});
    LANES;
    u32x4 w1;
    UNR for (int z = 0; z < 2; z++) {
      const float mult = (float)(z ? 2 * l31 + 1 : l31) * (1.0f / 64), b0 = (float)((8 * (wid & 3) + 4 * hh) * (2 - z) + 1 - z), ph = 0.25f * (float)(wid >> 2);
      u32x4 w;
      UNR for (int e = 0; e < 4; e++) { const float a = __builtin_fmaf(b0 + (float)(e * (2 - z)), mult, ph); w[e] = pk_bf16(cosr(a), cosr(a - 0.25f)); }
      if (z) LDS4(L_O2 + wid * 1024 + lane * 16) = w; else w1 = w; }
    TAB_TW(L_G1, 2, 1, (float)l31 * (1.0f / 2048));
    TAB_TW(L_TC, 1, 0, (float)(2 * l31 + 1) * (1.0f / 2048));
    UNR for (int i = 0; i < 4; i++) { FR[i] = LDS4(L_FR + i * 1024 + lane * 16);
      UNR for (int e = 0; e < 4; e++) FR[4 + i][e] = __builtin_amdgcn_alignbit(FR[i][e], FR[i][e], 16) ^ 0x8000u; }
    asm volatile("s_waitcnt vmcnt(%1)" : "+v"(w1) : "n"(16 - 8 * WN) : "memory"); BAR;
    __builtin_amdgcn_raw_buffer_store_b32(epoch, rsW, tid == 0 ? OFF_F1 + b * 128 : WS_USED, 0, 16);
    u32x4 sw[16];
    {
      const int rb = wid * 16896 + (l31 >> 1) * 1056 + 16 + hh * 32 + (lane & 1) * 16, sh = (lane & 2) * 8;
      WAIT_V(1);
      if (lane < 16) *(LAS u32x4u*)(lds + wid * 16896 + lane * 1056 + 1040 - WSH(lane & 7)) = wt;
      unsigned nx[16];
      UNR for (int j = 0; j < 16; j++) { sw[j] = LDS4(rb + j * 64); nx[j] = LDS1(rb + j * 64 + 16); }
      SCHED;
      UNR for (int j = 0; j < 16; j++) {
        sw[j] = (u32x4){__builtin_amdgcn_alignbit(sw[j][1], sw[j][0], sh), __builtin_amdgcn_alignbit(sw[j][2], sw[j][1], sh), __builtin_amdgcn_alignbit(sw[j][3], sw[j][2], sh), __builtin_amdgcn_alignbit(nx[j], sw[j][3], sh)};
        asm volatile("" : "+v"(sw[j])); } }
    if (WT && wid) { const int po_ = OFF_F1 + (wid * 16 + fr) * 128;
      _Pragma("unroll 1") for (unsigned sp = 0; sp < SPIN_CAP && __popcll(__ballot(FLAG_LD(po_) == epoch)) < 4 * WT; ++sp) __builtin_amdgcn_s_sleep(4); }
    { const int go = OFF_WT + hh * SLAB + (g * 8 + wid) * 512 + l31 * 16;
      UNR for (int j = 0; j < 16; j++) ST_SO(sw[j], go, 2 * j * SLAB); }
    if (wid == 0) _Pragma("unroll 1") for (unsigned sp = 0; sp < SPIN_CAP; ++sp) { unsigned d = 0;
      UNR for (int j = 0; j < 4; j++) d |= __builtin_amdgcn_raw_buffer_load_b32(rsW, OFF_F1 + lane * 128, j * 8192, 16) ^ epoch;
      if (__all(d == 0)) break;
      __builtin_amdgcn_s_sleep(2); asm volatile("" ::: "memory"); }
    BAR;
    LDS4(L_O1 + wid * 1024 + lane * 16) = w1;
    if (g == 0 && tid == 0) FLAG_ST(OFF_E);
    u32x4 sx[16]; u32x2 rp[4], rn[4];
    const int so = g * SLAB + wid * 16384 + lane * 16;
    UNR for (int e = 0; e < 4; e++) { const int j = l31 + 32 * (8 * wid + 4 * hh + e), o = g ? WS_USED : OFF_R + 8;
      rp[e] = __builtin_amdgcn_raw_buffer_load_b64(rsW, o + j * 16, 0, 16); rn[e] = __builtin_amdgcn_raw_buffer_load_b64(rsW, o + ((2048 - j) & 2047) * 16, 0, 16); }
    UNR for (int i = 0; i < 16; i++) sx[i] = LD_SC1(OFF_X1 + so + i * 1024);
    SCHED;
    WAIT_V(24); BAR;
    __builtin_amdgcn_raw_buffer_store_b32(epoch, rsW, tid < 32 ? OFF_FW + tid * 128 + g * 4 : WS_USED, 0, 16);
    if (g == 0) {
      u32x4 f1, f2;
      UNR for (int e = 0; e < 4; e++) {
        const float ar = bf_lo(rp[e][1]) + bf_lo(rn[e][1]), ai = bf_hi(rp[e][1]) - bf_hi(rn[e][1]), br = bf_lo(rp[e][0]) + bf_lo(rn[e][0]), bi = bf_hi(rp[e][0]) - bf_hi(rn[e][0]);
        f1[e] = pk_bf16(0.125f * (ar + br), -0.125f * (ai + bi)); f2[e] = pk_bf16(0.125f * (ar - br), 0.125f * (ai - bi)); }
      *(u32x4*)(ws + OFF_FF + tid * 16) = f1; *(u32x4*)(ws + OFF_FF + 8192 + tid * 16) = f2;
    }
    UNR for (int i = 0; i < 16; i++) { LDS4(wid * 16384 + i * 1024 + lane * 16) = sx[i]; SCHED; }
    WAIT_L(0); BAR;
    unsigned fw;
    BLD(fw, "buffer_load_dword", OFF_FW + g * 128 + l31 * 4, "sc1");
    { const int x0 = XBASE(0) + hh * 16384;
      UNR for (int j = 0; j < 4; j++) { const int b0 = opq(x0 + (j >> 1) * 2048 + (j & 1) * 65536), b1 = opq(b0 ^ 32);
        UNR for (int i = 0; i < 8; i++) { xc[2 * j + (i >> 2)][i & 3] = LDS1(b0 + (i >> 2) * 32768 + (i & 3) * 4096); xo[2 * j + (i >> 2)][i & 3] = LDS1(b1 + (i >> 2) * 32768 + (i & 3) * 4096); } } }
    asm volatile("s_waitcnt vmcnt(0)" : "+v"(fw)::"memory");
    if (!__all(fw == epoch)) POLL1(OFF_FW + g * 128 + l31 * 4);
    const int wo = opq(OFF_WT + g * SLAB + wid * 16384 + fq * 512 + fr * 16);
#define WGET(i0, n) do { UNR for (int i = (i0); i < (i0) + (n); i++) sw[i] = __builtin_amdgcn_raw_buffer_load_b128(rsW, wo + (i & 3) / 2 * 2048 + (i & 1) * 256, (i >> 2) * 4096, 16); SCHED; } while (0)
    WGET(0, 4);
    WAIT_L(0); BAR;
    int wsb[8];
    UNR for (int e = 0; e < 8; e++) wsb[e] = opq(wid * 16384 + (lane >> 1) * 64 + ((8 * (lane & 1) + e) ^ (lane >> 2)) * 4);
#define WPUT(i0) do { UNR for (int i = (i0); i < (i0) + 4; i += 2) { \
      UNR for (int e = 0; e < 8; e++) LDS1(wsb[e] + i * 1024) = __builtin_amdgcn_perm(sw[i + 1][e >> 1], sw[i][e >> 1], (e & 1) ? 0x07060302u : 0x05040100u); } SCHED; } while (0)
    const int ex = opq(L_EX + lane * 16) - L_EX;
#define STAGE_A(x, W0) do { f32x16 r0 = ZERO16, i0 = ZERO16, r1 = ZERO16, i1 = ZERO16; u32x4 tw[4]; \
      UNR for (int k = 0; k < 4; k++) { tw[k] = LDS4(ex + (L_T1 + k * 1024)); r0 = MF32(x[k], FR[k], r0); i0 = MF32(x[k], FR[4 + k], i0); } SCHED; \
      if (W0) WPUT(4); else WGET(4, 4); \
      UNR for (int q = 0; q < 4; q++) { r1 = MF32(x[4 + q], FR[q], r1); SCHED; TWQ(x[q], r0, i0, q, tw[q]); i1 = MF32(x[4 + q], FR[4 + q], i1); tw[q] = LDS4(ex + (L_G1 + q * 1024)); SCHED; if (!W0) WGET(8 + 2 * q, 2); } \
      if (W0) WPUT(8); \
      UNR for (int q = 0; q < 4; q++) { TWQ(x[4 + q], r1, i1, q, tw[q]); SCHED; } \
      WPUT(W0 ? 12 : 0); } while (0)
    STAGE_A(xc, 0); STAGE_A(xo, 8);
    WAIT_V(0); WAIT_L(0); BAR;
    }
#pragma unroll 1
    for (int c = 0; c < 2; c++) {
      LANES;
#define PF(i) do { if (E1F == 1 && ((i) < 3 || c == 0)) TOUCH(Wd + ((g * 8 + wid) * 7 + 4 * c + (i)) * 4096 + lane * 64, L_G1 + wid * 768); \
      if (E1F == 2 && !((i) & 1)) TOUCH(Wd + ((g * 8 + wid) * 7 + 3 * c + (i)) * 4096 + lane * 128, L_G1 + wid * 768); SCHED; } while (0)
      const int ex = opq(L_EX + lane * 16) - L_EX, xb0 = XBASE(c);
      const int wb[2] = {opq(xb0 + hh * 8192), opq(xb0 + hh * 8192 + 65536)};
      f32x16 er = ZERO16, ei = ZERO16, xr, xi, cr = ZERO16, ci = ZERO16;
      u32x4 bq[4], w0, w1, fp[8], tw[4];
#define C1_LOAD do { UNR for (int k = 0; k < 2; k++) { fp[k] = LDS4(ex + (L_O2 + k * 1024)); fp[4 + k] = LDS4(ex + (L_O2 + 4096 + k * 1024)); } tw[0] = LDS4(ex + L_T1); } while (0)
      { u32x4 fo[8];
        UNR for (int k = 0; k < 8; k++) fo[k] = LDS4(ex + (L_O1 + k * 1024));
        UNR for (int k = 0; k < 4; k++) { er = MF32(FR[k], xc[k], er); ei = MF32(FR[4 + k], xc[k], ei); }
        SCHED;
        UNR for (int e = 0; e < 4; e++) { w0[e] = LDS1(wb[0] + e * 2048); w1[e] = LDS1(wb[1] + e * 2048); }
        SCHED;
        xr = MF32(fo[0], xc[4], er); xi = MF32(fo[4], xc[4], ei);
        UNR for (int k = 1; k < 4; k++) { xr = MF32(fo[k], xc[4 + k], xr); xi = MF32(fo[4 + k], xc[4 + k], xi); }
        SCHED; }
      if (g == 0 && wid == 0 && c == 0) {
        u32x4 ff[16];
        UNR for (int q = 0; q < 16; q++) ff[q] = *(const u32x4*)(ws + OFF_FF + ((q & 3) * 4 + (q >> 2)) * 1024 + lane * 16);
        unsigned vp[2][16], vq[2][16];
        UNR for (int i = 0; i < 16; i++) { float a = __builtin_fmaf(2.f, er[i], -xr[i]), d = __builtin_fmaf(2.f, ei[i], -xi[i]); asm("" : "+v"(a)); asm("" : "+v"(d));
          vp[0][i] = pk_bf16(xr[i], xi[i]); vq[0][i] = pk_bf16(-xi[i], xr[i]); vp[1][i] = pk_bf16(a, d); vq[1][i] = pk_bf16(-d, a);
          LDS1(wb[0] + ((i & 3) + 8 * (i >> 2)) * 2048) = vp[0][i]; LDS1(wb[1] + ((i & 3) + 8 * (i >> 2)) * 2048) = vp[1][i]; }
        const int lm = (32 - l31) & 31, m0 = (32 - l31) * 64 + (lm >> 1) * 4 + (4 - 4 * hh) * 2048, mb[2] = {opq(m0 + 65536), opq(m0)};
        SCHED;
        UNR for (int q = 0; q < 4; q++) { u32x4 aq; unsigned mm[8], mn[8]; float yr[8], yi[8];
          UNR for (int j = 0; j < 8; j++) mm[j] = LDS1(j + q == 0 ? (lane == 0 ? 0 : mb[0] + 27 * 2048) : mb[j & 1] + (27 - (j >> 1) - 8 * q) * 2048);
          UNR for (int j = 0; j < 8; j++) mn[j] = __builtin_amdgcn_alignbit(mm[j], mm[j], 16) ^ 0x80000000u;
          SCHED;
          UNR for (int j = 0; j < 8; j++) { yr[j] = dot2(vp[j & 1][4 * q + (j >> 1)], ff[4 * q + (j & 1)][j >> 1], 0.f); yi[j] = dot2(vq[j & 1][4 * q + (j >> 1)], ff[4 * q + (j & 1)][j >> 1], 0.f); }
          UNR for (int j = 0; j < 8; j++) { yr[j] = dot2(mm[j], ff[4 * q + 2 + (j & 1)][j >> 1], yr[j]); yi[j] = dot2(mn[j], ff[4 * q + 2 + (j & 1)][j >> 1], yi[j]); }
          SCHED;
          UNR for (int e = 0; e < 4; e++) { aq[e] = pk_bf16(yr[2 * e] + yr[2 * e + 1], yi[2 * e] + yi[2 * e + 1]); bq[q][e] = pk_bf16(yr[2 * e] - yr[2 * e + 1], yi[2 * e] - yi[2 * e + 1]); }
          cr = MF32(aq, FR[q], cr); ci = MF32(aq, FR[4 + q], ci); SCHED; }
        C1_LOAD;
      } else {
        UNR for (int q = 0; q < 4; q++) { float ar[4], ai[4], br[4], bi[4], vr[4], vi[4], wl[4], wh[4]; u32x4 aq; const u32x4 u0 = w0, u1 = w1;
          if (q < 3) UNR for (int e = 0; e < 4; e++) { w0[e] = LDS1(wb[0] + (e + 8 * q + 8) * 2048); w1[e] = LDS1(wb[1] + (e + 8 * q + 8) * 2048); }
          else C1_LOAD;
          UNR for (int e = 0; e < 4; e++) { wl[e] = bf_lo(u0[e]); wh[e] = bf_hi(u0[e]); } SCHED;
          UNR for (int e = 0; e < 4; e++) { br[e] = xi[4 * q + e] * wh[e]; bi[e] = xi[4 * q + e] * wl[e]; } FIX8(br, bi); SCHED;
          UNR for (int e = 0; e < 4; e++) { vr[e] = __builtin_fmaf(2.f, er[4 * q + e], -xr[4 * q + e]); vi[e] = __builtin_fmaf(2.f, ei[4 * q + e], -xi[4 * q + e]); } SCHED;
          UNR for (int e = 0; e < 4; e++) { br[e] = __builtin_fmaf(xr[4 * q + e], wl[e], -br[e]); bi[e] = __builtin_fmaf(-xr[4 * q + e], wh[e], -bi[e]); } SCHED;
          UNR for (int e = 0; e < 4; e++) { wl[e] = bf_lo(u1[e]); wh[e] = bf_hi(u1[e]); } SCHED;
          UNR for (int e = 0; e < 4; e++) { ar[e] = __builtin_fmaf(-vi[e], wh[e], br[e]); ai[e] = __builtin_fmaf(-vi[e], wl[e], bi[e]); } SCHED;
          UNR for (int e = 0; e < 4; e++) { ar[e] = __builtin_fmaf(vr[e], wl[e], ar[e]); ai[e] = __builtin_fmaf(-vr[e], wh[e], ai[e]); } SCHED;
          UNR for (int e = 0; e < 4; e++) { br[e] = __builtin_fmaf(2.f, br[e], -ar[e]); bi[e] = __builtin_fmaf(2.f, bi[e], -ai[e]); }
          FIX8(ar, ai); FIX8(br, bi); SCHED;
          UNR for (int e = 0; e < 4; e++) aq[e] = pk_bf16(ar[e], ai[e]); SCHED;
          UNR for (int e = 0; e < 2; e++) bq[q][e] = pk_bf16(br[e], bi[e]); SCHED;
          cr = MF32(aq, FR[q], cr); SCHED;
          UNR for (int e = 2; e < 4; e++) bq[q][e] = pk_bf16(br[e], bi[e]);
          asm("" : "+v"(bq[q])); SCHED;
          ci = MF32(aq, FR[4 + q], ci); SCHED; }
      }
      PF(0);
      const int zb[2] = {opq(xb0 + hh * 16384), opq(xb0 + hh * 16384 + 65536)};
      f32x16 dr = ZERO16, di = ZERO16;
      er = ZERO16; ei = ZERO16; xr = ZERO16; xi = ZERO16;
      { UNR for (int k = 2; k < 4; k++) { fp[k] = LDS4(ex + (L_O2 + k * 1024)); fp[4 + k] = LDS4(ex + (L_O2 + 4096 + k * 1024)); }
        UNR for (int k = 1; k < 4; k++) tw[k] = LDS4(ex + (L_T1 + k * 1024));
        SCHED;
        UNR for (int k = 0; k < 2; k++) { er = MF32(bq[k], fp[k], er); ei = MF32(bq[k], fp[4 + k], ei); }
        SCHED;
        UNR for (int q = 0; q < 4; q++) { u32x4 up; TWQ(up, cr, ci, q, tw[q]); tw[q] = LDS4(ex + (L_TC + q * 1024)); SCHED;
          if (q & 1) ei = MF32(bq[2 + (q >> 1)], fp[6 + (q >> 1)], ei); else er = MF32(bq[2 + (q >> 1)], fp[2 + (q >> 1)], er);
          SCHED; dr = MF32(FR[q], up, dr); di = MF32(FR[4 + q], up, di); SCHED; if (q == 2) PF(1); }
        UNR for (int q = 0; q < 4; q++) { u32x4 up; TWQ(up, er, ei, q, tw[q]); SCHED;
          if (q) UNR for (int e = 0; e < 2; e++) LDS1(zb[(q - 1) >> 1] + ((q - 1) & 1) * 32768 + e * 4096) = pk_bf16(dr[4 * q - 4 + e], di[4 * q - 4 + e]);
          SCHED; xr = MF32(FR[q], up, xr); SCHED;
          if (q) UNR for (int e = 2; e < 4; e++) LDS1(zb[(q - 1) >> 1] + ((q - 1) & 1) * 32768 + e * 4096) = pk_bf16(dr[4 * q - 4 + e], di[4 * q - 4 + e]);
          SCHED; xi = MF32(FR[4 + q], up, xi); SCHED; if (q == 1) PF(2); }
        UNR for (int e = 0; e < 4; e++) LDS1(zb[1] + 32768 + e * 4096) = pk_bf16(dr[12 + e], di[12 + e]);
        UNR for (int k = 0; k < 8; k++) xc[k] = xo[k];
        SCHED; PF(3);
        UNR for (int i = 0; i < 16; i++) LDS1(zb[i >> 3] + ((i >> 2) & 1) * 32768 + (i & 3) * 4096 + 2048) = pk_bf16(xr[i], xi[i]); }
    }
    {
    LANES;
    WAIT_L(0); BAR;
    UNR for (int i = 0; i < 16; i++) ST_SC1(LDS4(wid * 16384 + i * 1024 + lane * 16), OFF_Z + g * SLAB + wid * 16384 + i * 1024 + lane * 16);
    WAIT_V(16);
    TAB_G1;
    WAIT_V(0); WAIT_L(0); BAR;
    if (tid < 32) FLAG_ST(OFF_F2 + tid * 128 + g * 4);
    { P3_ADDR; (void)zo_; P3_LOADX; }
    UNR for (int j = 0; j < 8; j++) asm volatile("" : "=v"(zd[j]));
    G2_PRELOAD;
    SCHED;
    }
  }
  if (!p3_early) {
    LANES;
    POLL1(OFF_F2 + rr * 128 + l31 * 4);
    P3_ADDR; (void)xo_; UNR for (int j = 0; j < 8; j++) P3_LOADZ(j, "+v"); if (!PL) P3_WAIT(0);
    if (!isG) { WAIT_V(8); BAR; }
  }
  {
    LANES;
    const int kg = wid >> 2, rg = wid & 3;
    unsigned fb;
    const int fo = opq(OFF_FH + (tile * 2 + half) * 128 + (lane < np ? lane : 0) * 4);
    const int va0 = opq(OFF_H + ((tm * 8 + rg * 2 + kg) * 112 + kt0 * 2 + kg) * 1024 + lane * 16), va1 = opq(OFF_H + ((tm * 8 + rg * 2 + (kg ^ 1)) * 112 + kt0 * 2 + kg) * 1024 + lane * 16);
    u32x4 Ar[4][2], ev[4]; u32x2 B0[8][2], B1[8][2];
    BLD(fb, "buffer_load_dword", fo, "sc1");
    u32x4 frr[4], up[4], g1[4], tw3[4];
    { const int ex = opq(L_EX + lane * 16) - L_EX, ea = opq(L_EX + ((lane & 51) | ((lane & 4) << 1) | ((lane & 8) >> 1)) * 16) - L_EX;
      UNR for (int i = 0; i < 4; i++) g1[i] = LDS4(ex + (L_G1 + i * 1024));
      UNR for (int i = 0; i < 4; i++) tw3[i] = LDS4(ea + (L_T1 + i * 1024));
      UNR for (int i = 0; i < 4; i++) frr[i] = LDS4(ex + (L_FR + i * 1024)); }
#define P3A { const float sgn = hh ? 0.f : ((lane & 1) ? 0x1p-20f : -0x1p-20f); \
      const u32x4 zz[2] = {{zd[0], zd[1], zd[2], zd[3]}, {zd[4], zd[5], zd[6], zd[7]}}; \
      f32x16 re = MF32(zz[0], g1[0], ZERO16), im = MF32(zz[0], g1[2], ZERO16); \
      re = MF32(zz[1], g1[1], re); im = MF32(zz[1], g1[3], im); \
      const unsigned zq = __builtin_amdgcn_readlane(zd[0], 0); \
      re[0] = __builtin_fmaf(sgn, bf_hi(zq), re[0]); \
      UNR for (int i = 0; i < 16; i++) { float a_, c_; cmul(re[i], im[i], tw3[i >> 2][i & 3], a_, c_); up[i >> 2][i & 3] = pk_bf16(a_, c_); } }
    if (PL == 2) P3_WAIT(1);
    if (PL != 1) { P3A; asm volatile("" : "+v"(up[0]), "+v"(up[1]), "+v"(up[2]), "+v"(up[3])); }
    unsigned voffD; G2_VOFF; voffD = opq(voffD);
    int bo[8];
    { int q = fr >> 2, p = lane & 3; int row = 32 * kg + 16 * (fq >> 1) + 4 * (fq & 1) + q; int base = row * 256 + (((p >> 1) ^ ((row & 7) << 1)) * 16) + 8 * (p & 1);
      UNR for (int n = 0; n < 8; n++) bo[n] = opq(base ^ (n * 32)); }
    const int to = opq(OFF_T + tok * 2048 + fr * 64 + hh * 16 + (fq & 1) * 32);
    f32x4 acc[2][8]; int E1 = 0;
    asm volatile("s_waitcnt vmcnt(0)" : "+v"(fb)::"memory");
    P3_WAIT(0);
#define SPIN(o) _Pragma("unroll 1") for (unsigned sp = 0; sp < SPIN_CAP && !__all(fb == epoch); ++sp) { \
      __builtin_amdgcn_s_sleep(SLP); BLD(fb, "buffer_load_dword", o, "sc1"); asm volatile("s_waitcnt vmcnt(0)" : "+v"(fb)::"memory"); }
    SPIN(fo);
#define LDH1(sl, t, i) asm volatile("buffer_load_dwordx4 %0, %1, %2, %3 offen sc1" : "=v"(Ar[sl][i]) : "v"((i) ? va1 : va0), "s"(rsW), "s"(KT(t) * 2048) : "memory")
#define LDE(c, o) UNR for (int u = 0; u < 4; u++) asm volatile("buffer_load_dwordx4 %0, %1, %2, %3 offen sc1" : c(ev[u]) : "v"(o), "s"(rsW), "s"(u * es) : "memory")
#define WAIT_LD(B) asm volatile("s_waitcnt lgkmcnt(0)" : "+v"(B[0][0]), "+v"(B[0][1]), "+v"(B[1][0]), "+v"(B[1][1]), "+v"(B[2][0]), "+v"(B[2][1]), "+v"(B[3][0]), "+v"(B[3][1]), \
   "+v"(B[4][0]), "+v"(B[4][1]), "+v"(B[5][0]), "+v"(B[5][1]), "+v"(B[6][0]), "+v"(B[6][1]), "+v"(B[7][0]), "+v"(B[7][1])::"memory")
#define WAIT_VA(n_, sl) asm volatile("s_waitcnt vmcnt(%2)" : "+v"(Ar[sl][0]), "+v"(Ar[sl][1]) : "n"(n_) : "memory")
#define FLIP do { UNR for (int n = 0; n < 8; n++) bo[n] ^= 65536; } while (0)
#define ITER(t, Bc, Bn, sl, so, VC, ISB, ISA, RD, DOBAR) do { WAIT_VA(VC, sl); WAIT_LD(Bc); if (DOBAR) BAR; SCHED; \
    UNR for (int n = 0; n < 8; n++) { \
      if (RD) { TRR(Bn[n][0], bo[n], so); TRR(Bn[n][1], bo[n], so + 2048); } \
      acc[n & 1][n] = MF16(CAT(Bc[n]), Ar[sl][n & 1], acc[n & 1][n]); SCHED; \
      acc[(n & 1) ^ 1][n] = MF16(CAT(Bc[n]), Ar[sl][(n & 1) ^ 1], acc[(n & 1) ^ 1][n]); SCHED; \
      if (ISB && n == 0) STD1((t) + 7, 0); if (ISB && n == 2) STD1((t) + 7, 1); \
      if (ISA == 1 && n == 4) LDH1(((sl) + 3) & 3, (t) + 3, 0); if (ISA == 1 && n == 6) LDH1(((sl) + 3) & 3, (t) + 3, 1); \
      if (ISA == 2 && n < 4) asm volatile("buffer_load_dwordx4 %0, %1, %2, %3 offen sc1" : "=v"(ev[n & 3]) : "v"(E1), "s"(rsW), "s"(n * es) : "memory"); SCHED; } } while (0)
    LDH1(0, 0, 0); LDH1(0, 0, 1); LDH1(1, 1, 0); LDH1(1, 1, 1); LDH1(2, 2, 0); LDH1(2, 2, 1);
    SCHED;
    if (PL == 1) P3A;
    { f32x16 y;
      UNR for (int i = 0; i < 8; i++) { y[2 * i] = bf_lo(zx[i >> 2][i & 3]); y[2 * i + 1] = bf_hi(zx[i >> 2][i & 3]); }
      UNR for (int k = 0; k < 4; k++) y = MF32(up[k], frr[k], y);
      u32x4 o[2];
      UNR for (int i = 0; i < 8; i++) o[i >> 2][i & 3] = pk_bf16(y[2 * i], y[2 * i + 1]);
      UNR for (int e = 0; e < 4; e++) { auto sw = __builtin_amdgcn_permlane16_swap(o[0][e], o[1][e], false, false); o[0][e] = sw[0]; o[1][e] = sw[1]; }
      ST_SC1(o[0], to); ST_SC1(o[1], to + 1024); }
    const int row = tm * 128 + (rg * 2 + kg) * 16 + fr, colo = tn * 128 + (fq & 1) * 16 + (fq >> 1) * 8;
    const int po = OFF_P + tile * 32768 + wid * 4096 + lane * 16;
    const int eo = half ? OFF_T + row * (D_ * 2) + colo * 2 : po, es = half ? 64 : 1024;
    UNR for (int a = 0; a < 2; a++) UNR for (int n = 0; n < 8; n++) acc[a][n] = (f32x4){0, 0, 0, 0};
    SCHED;
    UNR for (int n = 0; n < 8; n++) { TRR(B0[n][0], bo[n], 0); TRR(B0[n][1], bo[n], 2048); }
    ITER(0, B0, B1, 0, 16384, 6, 0, 1, 1, 1);
    if (isG && wid == 0) HFLAG(OFF_FB);
    ITER(1, B1, B0, 1, 32768, 6, 1, 1, 1, 0);
    ITER(2, B0, B1, 2, 49152, 8, 1, 1, 1, 1); FLIP;
    BLD(fb, "buffer_load_dword", WS_USED, "sc1");
    ITER(3, B1, B0, 3, 0, 9, 1, 1, 1, 0);
    int t;
    for (t = 4; t < nkt - 8; t += 4) {
      ITER(t, B0, B1, 0, 16384, 9, 1, 1, 1, 1);
      asm volatile("s_waitcnt vmcnt(8)" : "+v"(fb)::"memory");
      if (t + tid == 4) FLAG_ST(OFF_F3 + b * 128);
      if (t == np - 4) { SPIN(fo + (OFF_FB - OFF_FH)); }
      ITER(t + 1, B1, B0, 1, 32768, 8, 1, 1, 1, 0);
      ITER(t + 2, B0, B1, 2, 49152, 8, 1, 1, 1, 1); FLIP;
      BLD(fb, "buffer_load_dword", t == np - 8 ? fo + (OFF_FB - OFF_FH) : WS_USED, "sc1");
      ITER(t + 3, B1, B0, 3, 0, 9, 1, 1, 1, 0); }
    ITER(t, B0, B1, 0, 16384, 8, 1, 1, 1, 1);
    asm volatile("s_waitcnt vmcnt(8)" : "+v"(fb)::"memory");
    ITER(t + 1, B1, B0, 1, 32768, 8, 0, 1, 1, 0);
    ITER(t + 2, B0, B1, 2, 49152, 6, 0, 1, 1, 1); FLIP; ITER(t + 3, B1, B0, 3, 0, 4, 0, 1, 1, 0);
    const int fe = opq(half ? OFF_F3 + (16 * tm + fr) * 128 : OFF_FP + tile * 128);
#define SAMPLE(n) do { asm volatile("s_waitcnt vmcnt(" #n ")" : "+v"(fb)::"memory"); got = __all(fb == epoch); BLD(fb, "buffer_load_dword", got ? WS_USED : fe, "sc1"); } while (0)
    BLD(fb, "buffer_load_dword", fe, "sc1");
    ITER(t + 4, B0, B1, 0, 16384, 5, 0, 1, 1, 1); ITER(t + 5, B1, B0, 1, 32768, 5, 0, 0, 1, 0);
    bool got;
    SAMPLE(2);
    E1 = got ? eo : WS_USED;
    ITER(t + 6, B0, B1, 2, 49152, 3, 0, 2, 1, 1);
    const int wo = opq(wid * 8192 + lane * 16);
    WAIT_VA(5, 3); WAIT_LD(B1); BAR; SCHED;
    if (!got) { SAMPLE(4); if (got) LDE("+v", eo); }
    UNR for (int n = 0; n < 8; n++) { acc[1][n] = MF16(CAT(B1[n]), Ar[3][1], acc[1][n]); SCHED; if (n > 1) { *(LAS f32x4*)(lds + wo + (n - 2) * 1024) = acc[1][n - 2]; SCHED; } }
    UNR for (int n = 0; n < 8; n++) { acc[0][n] = MF16(CAT(B1[n]), Ar[3][0], acc[0][n]); SCHED; if (n < 2) { *(LAS f32x4*)(lds + wo + (n + 6) * 1024) = acc[1][n + 6]; SCHED; } }
    if (!got) { asm volatile("s_waitcnt vmcnt(0)" : "+v"(fb)::"memory"); SPIN(fe); LDE("+v", eo); }
    UNR for (int u = 0; u < 4; u++) UNR for (int j = 0; j < 4; j++) {
      auto sw = __builtin_amdgcn_permlane16_swap(__float_as_uint(acc[0][2 * u][j]), __float_as_uint(acc[0][2 * u + 1][j]), false, false);
      acc[0][2 * u][j] = __uint_as_float(sw[0]); acc[0][2 * u + 1][j] = __uint_as_float(sw[1]); }
    const int oo = opq(half ? po : row * (D_ * 2) + colo * 2), xo = opq((wid ^ 4) * 8192 + (fq & 1) * 1024 + ((fq & 2) * 16 + fr) * 16);
    const auto rsO = __builtin_amdgcn_make_buffer_rsrc(half ? ws : O, 0, half ? WS_USED : S_ * D_ * 2, 0x00020000);
    SCHED; WAIT_L(0); BAR;
    UNR for (int n = 0; n < 8; n++) acc[1][n] = *(LAS f32x4*)(lds + xo + (n >> 1) * 2048 + (n & 1) * 256);
    SCHED;
    asm volatile("s_waitcnt vmcnt(0)" : "+v"(fb), "+v"(ev[0]), "+v"(ev[1]), "+v"(ev[2]), "+v"(ev[3])::"memory");
    UNR for (int n = 0; n < 8; n++) { const unsigned e0 = ev[n >> 1][2 * (n & 1)], e1 = ev[n >> 1][2 * (n & 1) + 1]; acc[0][n] += (f32x4){bf_lo(e0), bf_hi(e0), bf_lo(e1), bf_hi(e1)}; }
    SCHED;
    UNR for (int u = 0; u < 4; u++) { const f32x4 lo = acc[0][2 * u] + acc[1][2 * u], hi = acc[0][2 * u + 1] + acc[1][2 * u + 1];
      const u32x4 w = {pk_bf16(lo[0], lo[1]), pk_bf16(lo[2], lo[3]), pk_bf16(hi[0], hi[1]), pk_bf16(hi[2], hi[3])};
      asm volatile("buffer_store_dwordx4 %0, %1, %2, %3 offen sc1\n\ts_nop 1" :: "v"(w), "v"(oo), "s"(rsO), "s"(u * (1088 - es)) : "memory"); SCHED; }
    if (half) { WAIT_V(0); BAR; if (tid == 0) FLAG_ST(OFF_FP + tile * 128); }
  }
}

extern "C" void kernel_launch(void* const* d_in, const int* in_sizes, int n_in, void* d_out, int out_size, void* d_ws, size_t ws_size, hipStream_t stream) {
  (void)in_sizes; (void)n_in; (void)out_size; (void)ws_size;
  (void)hipFuncSetAttribute((const void*)fused_k, hipFuncAttributeMaxDynamicSharedMemorySize, LDS_BYTES);
  fused_k<<<256, 512, LDS_BYTES, stream>>>((const char*)d_in[0], (const char*)d_in[1], (const char*)d_in[2], (const char*)d_in[4],
                                           (const char*)d_in[5], (const char*)d_in[6], (char*)d_out, (char*)d_ws);
}
```
